```python
import torch

def _softmax(x, axis=-1):
    return torch.softmax(x, dim=axis)
def _gelu(x):
    return torch.nn.functional.gelu(x, approximate="tanh")
def _silu(x):
    return torch.nn.functional.silu(x)
def _sigmoid(x):
    return torch.sigmoid(x)
def _layernorm(x, axis=-1, eps=1e-5):
    m = x.mean(dim=axis, keepdim=True)
    v = x.var(dim=axis, keepdim=True, unbiased=False)
    return (x - m) / torch.sqrt(v + eps)
def _scatter_add(tgt, idx, src, axis=0):
    return tgt.clone().index_add_(axis, idx, src)
def _segment_sum(src, seg, n):
    out = torch.zeros((n,) + src.shape[1:], dtype=src.dtype)
    return out.index_add_(0, seg, src)
def _pad(x, pw):
    flat = tuple(v for pair in reversed(pw) for v in pair)
    return torch.nn.functional.pad(x, flat)

kernel_name = 'recurrent_scan__ssm_conv_s5__dense_activation_relu__prompt'

def setup_inputs():

    _w = (lambda a: (lambda t: t.to(torch.bfloat16) if t.is_floating_point() else t)(torch.as_tensor(a)))
    def _A_setup():
        B, S, D = 1, 2048, 640
        P = D
        Lambda_re = -0.5 * (lambda _x: torch.exp(torch.as_tensor(_x)))((lambda *s: torch.randn(*s, dtype=torch.bfloat16) * 0.2)(P))
        Lambda_im = (lambda *s: torch.randn(*s, dtype=torch.bfloat16) * 0.4)(P) * 1.0
        B_re = (lambda *s: torch.randn(*s, dtype=torch.bfloat16) * 0.2)(P, D) * (D ** -0.5)
        B_im = (lambda *s: torch.randn(*s, dtype=torch.bfloat16) * 0.2)(P, D) * (D ** -0.5)
        C_re = (lambda *s: torch.randn(*s, dtype=torch.bfloat16) * 0.2)(D, P) * (P ** -0.5)
        C_im = (lambda *s: torch.randn(*s, dtype=torch.bfloat16) * 0.2)(D, P) * (P ** -0.5)
        log_dt = (lambda *s: torch.randn(*s, dtype=torch.bfloat16) * 0.2)(P) * 0.3 - 2.0
        Dskip = (lambda *s: torch.randn(*s, dtype=torch.bfloat16) * 0.05)(D)
        return {
            "x": _w((lambda *s: torch.randn(*s, dtype=torch.bfloat16) * 0.05)(B, S, D)),
            "Lambda_re": _w(Lambda_re),
            "Lambda_im": _w(Lambda_im),
            "B_re": _w(B_re),
            "B_im": _w(B_im),
            "C_re": _w(C_re),
            "C_im": _w(C_im),
            "log_dt": _w(log_dt),
            "Dskip": _w(Dskip),
        }
    _A_in = _A_setup()

    def _B_setup():
        B, S, D, Dff = 1, 2048, 640, 1728
        return {
            "x": _w((lambda *s: torch.randn(*s, dtype=torch.bfloat16) * 0.05)(B, S, D)),
            "w1": _w((lambda *s: torch.randn(*s, dtype=torch.bfloat16) * 0.05)(D, Dff)),
            "w2": _w((lambda *s: torch.randn(*s, dtype=torch.bfloat16) * 0.05)(Dff, D)),
        }
    _B_in = _B_setup()

    return {
        "A_x": _A_in["x"],
        "A_Lambda_re": _A_in["Lambda_re"],
        "A_Lambda_im": _A_in["Lambda_im"],
        "A_B_re": _A_in["B_re"],
        "A_B_im": _A_in["B_im"],
        "A_C_re": _A_in["C_re"],
        "A_C_im": _A_in["C_im"],
        "A_log_dt": _A_in["log_dt"],
        "A_Dskip": _A_in["Dskip"],
        "B_x": _B_in["x"],
        "B_w1": _B_in["w1"],
        "B_w2": _B_in["w2"],
    }

def reference(A_x, A_Lambda_re, A_Lambda_im, A_B_re, A_B_im, A_C_re, A_C_im, A_log_dt, A_Dskip, B_x, B_w1, B_w2):
    def _A_ref(x, Lambda_re, Lambda_im, B_re, B_im, C_re, C_im, log_dt, Dskip):
        Bsz, L, H = x.shape
        Lam = Lambda_re + 1j * Lambda_im
        Bmat = B_re + 1j * B_im
        Cmat = C_re + 1j * C_im
        dt = torch.exp(log_dt)
        Lam_bar = torch.exp(dt * Lam)
        B_bar = ((Lam_bar - 1.0) / Lam)[:, None] * Bmat
        Bu = torch.einsum("bld,pd->blp", x + 0j, B_bar)
        h = torch.zeros_like(Bu[:, 0, :])
        hs = []
        for t in range(L):
            h = Lam_bar[None, :] * h + Bu[:, t, :]
            hs.append(h)
        hseq = torch.stack(hs, 1)
        y = 2.0 * torch.einsum("blp,dp->bld", hseq, Cmat).real + x * Dskip[None, None, :]
        return y

    def _B_ref(x, w1, w2):
        h = torch.einsum("bsd,df->bsf", x, w1)
        zero = torch.zeros_like(h)
        h = torch.maximum(h, zero)
        return torch.einsum("bsf,fd->bsd", h, w2)

    x = A_x
    xn = _layernorm(x, axis=-1)
    s = _A_ref(xn, A_Lambda_re, A_Lambda_im, A_B_re, A_B_im, A_C_re, A_C_im, A_log_dt, A_Dskip)
    g = _silu(_B_ref(xn, B_w1, B_w2))
    return x + s * g
```

```cpp
#include <hip/hip_runtime.h>
#include <hip/hip_bf16.h>
#include <stdint.h>
typedef unsigned short u16;
typedef __attribute__((ext_vector_type(8))) short bf16x8;
typedef __attribute__((ext_vector_type(4))) float f32x4;
#define L_ 2048
#define D_ 640
#define P_ 640
#define F_ 1728
__device__ __forceinline__ float bf2f(u16 v){ return __uint_as_float(((unsigned)v)<<16); }
__device__ __forceinline__ u16 f2bf(float f){ unsigned u=__float_as_uint(f); u+=0x7fffu+((u>>16)&1u); return (u16)(u>>16); }
__device__ __forceinline__ float rbf(float f){ return bf2f(f2bf(f)); }

__global__ void ln_k(const u16* x,u16* xn){
  int row=blockIdx.x*4+(threadIdx.x>>6), l=threadIdx.x&63;
  float v[10]; float s=0;
  for(int i=0;i<10;i++){ v[i]=bf2f(x[row*D_+l+64*i]); s+=v[i]; }
  for(int o=32;o>0;o>>=1) s+=__shfl_xor(s,o);
  float mean=s*(1.f/D_);
  float q=0; for(int i=0;i<10;i++){ float d=v[i]-mean; q+=d*d; }
  for(int o=32;o>0;o>>=1) q+=__shfl_xor(q,o);
  float var=rbf(q*(1.f/D_)); float m=rbf(mean);
  float sd=rbf(sqrtf(rbf(var+1e-5f)));
  for(int i=0;i<10;i++){ xn[row*D_+l+64*i]=f2bf(rbf(v[i]-m)/sd); }
}
template<int MODE>
__global__ void gemm_k(const u16* A,int lda,const u16* B,int sbk,int sbn,int K,int N,float* Cf,u16* Cb,int ldc){
  int wave=blockIdx.x*4+(threadIdx.x>>6), l=threadIdx.x&63;
  int ntn=N/16; int tm=wave/ntn, tn=wave%ntn;
  int r=l&15,g=l>>4;
  f32x4 acc={0,0,0,0};
  for(int k=0;k<K;k+=32){
    bf16x8 a=*(const bf16x8*)(A+(size_t)(tm*16+r)*lda+k+8*g);
    bf16x8 b;
    for(int j=0;j<8;j++) b[j]=(short)B[(size_t)(k+8*g+j)*sbk+(size_t)(tn*16+r)*sbn];
    acc=__builtin_amdgcn_mfma_f32_16x16x32_bf16(a,b,acc,0,0,0);
  }
  for(int j=0;j<4;j++){ int row=tm*16+g*4+j, col=tn*16+r;
    if(MODE==0) Cf[(size_t)row*ldc+col]=acc[j];
    if(MODE==1) Cb[(size_t)row*ldc+col]=f2bf(fmaxf(acc[j],0.f));
    if(MODE==2) Cf[(size_t)row*ldc+col]+=acc[j];
  }
}
__global__ void scan_k(const float* ure,const float* uim,const u16* lre,const u16* lim,const u16* ldt,u16* hre,u16* him){
  int p=blockIdx.x*64+threadIdx.x;
  float dt=rbf(expf(bf2f(ldt[p]))); float lr=bf2f(lre[p]), li=bf2f(lim[p]);
  float ar=dt*lr, ai=dt*li; float e=expf(ar); float br=e*cosf(ai), bi=e*sinf(ai);
  float nr=br-1.f, ni=bi; float den=lr*lr+li*li; float cr=(nr*lr+ni*li)/den, ci=(ni*lr-nr*li)/den;
  float hr=0,hi=0;
  for(int t=0;t<L_;t++){ float a=ure[t*P_+p], b=uim[t*P_+p];
    float ur=cr*a-ci*b, ui=cr*b+ci*a;
    float nhr=br*hr-bi*hi+ur, nhi=br*hi+bi*hr+ui; hr=nhr; hi=nhi;
    hre[t*P_+p]=f2bf(2.f*hr); him[t*P_+p]=f2bf(-2.f*hi); }
}
__global__ void fin_k(const u16* x,const u16* xn,const u16* dsk,const float* y,const float* f,float* out){
  int i=blockIdx.x*256+threadIdx.x; int d=i%D_;
  float s=y[i]+rbf(bf2f(xn[i])*bf2f(dsk[d]));
  float fv=rbf(f[i]); float g=rbf(fv/(1.f+expf(-fv)));
  out[i]=bf2f(x[i])+s*g;
}
extern "C" void kernel_launch(void* const* d_in, const int* in_sizes, int n_in, void* d_out, int out_size, void* d_ws, size_t ws_size, hipStream_t stream){
  const u16* x=(const u16*)d_in[0]; const u16* lre=(const u16*)d_in[1]; const u16* lim=(const u16*)d_in[2];
  const u16* bre=(const u16*)d_in[3]; const u16* bim=(const u16*)d_in[4]; const u16* cre=(const u16*)d_in[5]; const u16* cim=(const u16*)d_in[6];
  const u16* ldt=(const u16*)d_in[7]; const u16* dsk=(const u16*)d_in[8]; const u16* w1=(const u16*)d_in[10]; const u16* w2=(const u16*)d_in[11];
  char* ws=(char*)d_ws;
  u16* xn=(u16*)ws; ws+=L_*D_*2;
  float* ure=(float*)ws; ws+=L_*P_*4; float* uim=(float*)ws; ws+=L_*P_*4;
  u16* h1=(u16*)ws; ws+=L_*F_*2;
  u16* hre=(u16*)ws; ws+=L_*P_*2; u16* him=(u16*)ws; ws+=L_*P_*2;
  float* y=(float*)ws; ws+=L_*D_*4; float* f=(float*)ws; ws+=L_*D_*4;
  ln_k<<<L_/4,256,0,stream>>>(x,xn);
  gemm_k<0><<<(L_/16)*(P_/16)/4,256,0,stream>>>(xn,D_,bre,1,D_,D_,P_,ure,nullptr,P_);
  gemm_k<0><<<(L_/16)*(P_/16)/4,256,0,stream>>>(xn,D_,bim,1,D_,D_,P_,uim,nullptr,P_);
  gemm_k<1><<<(L_/16)*(F_/16)/4,256,0,stream>>>(xn,D_,w1,F_,1,D_,F_,nullptr,h1,F_);
  scan_k<<<P_/64,64,0,stream>>>(ure,uim,lre,lim,ldt,hre,him);
  gemm_k<0><<<(L_/16)*(D_/16)/4,256,0,stream>>>(hre,P_,cre,1,P_,P_,D_,y,nullptr,D_);
  gemm_k<2><<<(L_/16)*(D_/16)/4,256,0,stream>>>(him,P_,cim,1,P_,P_,D_,y,nullptr,D_);
  gemm_k<0><<<(L_/16)*(D_/16)/4,256,0,stream>>>(h1,F_,w2,D_,1,F_,D_,f,nullptr,D_);
  fin_k<<<L_*D_/256,256,0,stream>>>(x,xn,dsk,y,f,(float*)d_out);
}
```

```cpp
#include <hip/hip_runtime.h>
#include <stdint.h>

typedef unsigned short u16;
typedef __attribute__((ext_vector_type(8))) short bf16x8;
typedef __attribute__((ext_vector_type(4))) short s16x4;
typedef __attribute__((ext_vector_type(4))) float f32x4;
typedef __attribute__((ext_vector_type(2))) float f32x2;
typedef __attribute__((ext_vector_type(4))) unsigned u32x4;
typedef __attribute__((ext_vector_type(2))) __bf16 bf16x2;
#define LDS_AS __attribute__((address_space(3)))
#define DI __device__ __forceinline__
#define MFMA(a, b, c) __builtin_amdgcn_mfma_f32_16x16x32_bf16((a), (b), (c), 0, 0, 0)
#define SB() __builtin_amdgcn_sched_barrier(0)

constexpr int L_ = 2048, D_ = 640, P_ = 640, F_ = 1728;
constexpr int KSF = F_ / 32;
constexpr int KSY = P_ / 16;

constexpr size_t WS_H1F = 0;
constexpr size_t WS_HLOC = WS_H1F + (size_t)32 * KSF * 4096;
constexpr size_t WS_HF = WS_HLOC + (size_t)32 * KSY * 4096;
constexpr size_t WS_W2F = WS_HF + (size_t)32 * KSY * 4096;
constexpr size_t WS_CF = WS_W2F + (size_t)8 * KSF * 5120;
constexpr size_t WS_PPRE = WS_CF + (size_t)8 * KSY * 5120;
constexpr size_t WS_E64 = WS_PPRE + (size_t)32 * KSY * 512;
constexpr size_t WS_STAT = WS_E64 + (size_t)KSY * 32 * 128;
constexpr size_t WS_CONST = WS_STAT + (size_t)L_ * 8;
constexpr size_t WS_FLAG = WS_CONST + (size_t)P_ * 16;
constexpr size_t WS_END = WS_FLAG + 32 * 128;

DI float bf2f(u16 v) { return __uint_as_float(((unsigned)v) << 16); }
DI unsigned pk_bf16(float lo, float hi) {
  f32x2 f = {lo, hi};
  return __builtin_bit_cast(unsigned, __builtin_convertvector(f, bf16x2));
}
DI float rbf(float f) { return __uint_as_float(pk_bf16(f, 0.f) << 16); }
DI float lo_f(unsigned u) { return __uint_as_float(u << 16); }
DI float hi_f(unsigned u) { return __uint_as_float(u & 0xffff0000u); }
DI f32x2 cmul(f32x2 a, f32x2 b) { return (f32x2){a[0] * b[0] - a[1] * b[1], a[0] * b[1] + a[1] * b[0]}; }
DI f32x2 cfma(f32x2 a, f32x2 b, f32x2 c) {
  return (f32x2){__builtin_fmaf(a[0], b[0], __builtin_fmaf(-a[1], b[1], c[0])),
                 __builtin_fmaf(a[0], b[1], __builtin_fmaf(a[1], b[0], c[1]))};
}
DI bf16x8 tr_frag(const char* base, int off0, int off1) {
  s16x4 lo = __builtin_amdgcn_ds_read_tr16_b64_v4i16((LDS_AS s16x4*)(base + off0));
  s16x4 hi = __builtin_amdgcn_ds_read_tr16_b64_v4i16((LDS_AS s16x4*)(base + off1));
  return __builtin_shufflevector(lo, hi, 0, 1, 2, 3, 4, 5, 6, 7);
}
template <int OFF>
DI s16x4 tr_asm(unsigned addr) {
  s16x4 v;
  asm volatile("ds_read_b64_tr_b16 %0, %1 offset:%2" : "=v"(v) : "v"(addr), "i"(OFF));
  return v;
}
typedef __amdgpu_buffer_rsrc_t rsrc_t;
DI rsrc_t mk_rsrc(const void* p, unsigned bytes) {
  return __builtin_amdgcn_make_buffer_rsrc((void*)p, 0, bytes, 0x00020000);
}
DI void glds16(rsrc_t r, char* lds, int voff, int soff) {
  __builtin_amdgcn_raw_ptr_buffer_load_lds(r, (LDS_AS void*)lds, 16, voff, soff, 0, 0);
}
template <int V> struct IC { static constexpr int value = V; };
#define WAIT_VM(n) asm volatile("s_waitcnt vmcnt(" #n ")" ::: "memory")

#define STAMP(ws, kern, i)
struct K1Args {
  const u16 *x, *lre, *lim, *bre, *bim, *cre, *cim, *ldt, *w1, *w2;
  char* ws;
};

constexpr int K1_SLOT = 40960;
constexpr int K1_PT_W2 = 3 * K1_SLOT;
constexpr int K1_PT_C = K1_PT_W2 + 15360;
constexpr int K1_LDS = K1_PT_C + 15360;

template <bool BU>
DI void k1_body(const K1Args& a, char* sm, int ti, int tj) {
  const int tid = threadIdx.x, l = tid & 63;
  const int w = __builtin_amdgcn_readfirstlane(tid >> 6);
  const int wm = w >> 1, wn = w & 1;
  const int r = l & 15, g = l >> 4;
  const int l3 = l >> 3, l7 = l & 7;
  const int jb = tj - 9;
  STAMP(a.ws, 0, 0);

  const rsrc_t rx = mk_rsrc(a.x + (size_t)ti * 128 * D_, 128 * D_ * 2);
  int vx;
  {
    const int rr = 8 * (w & 1) + l3;
    const int row = BU ? (16 * (rr >> 2) + 4 * (w >> 1) + (rr & 3)) : (8 * w + l3);
    vx = row * (D_ * 2) + ((l7 ^ l3) << 4);
  }
  rsrc_t rw;
  int vw0, vw1 = 0, qmax = 5;
  if (BU) {
    rw = mk_rsrc((w >> 1) ? a.bim : a.bre, P_ * D_ * 2);
    vw0 = (96 * jb + 8 * (w & 1) + l3) * (D_ * 2) + ((l7 ^ l3) << 4);
    qmax = (jb == 6) ? 3 : 5;
  } else {
    rw = mk_rsrc(a.w1, D_ * F_ * 2);
    const int f0 = (l3 >> 1) & 1;
    const int base = (16 * w + l3) * (F_ * 2) + tj * 384 + (l7 & 1) * 16;
    vw0 = base + (((l7 >> 1) ^ f0) << 5);
    vw1 = base + (((l7 >> 1) ^ (f0 | 2)) << 5);
  }
  auto stage_piece = [&](int s, int q) {
    char* slot = sm + (s % 3) * K1_SLOT;
    if (q < 4) {
      const int rowoff = BU ? (64 * (q >> 1) + 8 * (q & 1)) : 32 * q;
      glds16(rx, slot + (4 * q + w) * 1024, vx, rowoff * (D_ * 2) + s * 128);
    } else {
      const int qq = q - 4;
      if (BU) {
        const int qe = qq < qmax ? qq : qmax;
        glds16(rw, slot + 16384 + (4 * qq + w) * 1024, vw0, qe * (16 * D_ * 2) + s * 128);
      } else {
        glds16(rw, slot + 16384 + (6 * w + qq) * 1024, qq < 3 ? vw0 : vw1,
               (s * 64 + 8 * (qq / 3)) * (F_ * 2) + (qq % 3) * 128);
      }
    }
  };

  const int hb = ti * 9 + tj;
  const int pcg = hb & 7, pgrp = hb >> 3;
  const int cks0 = pgrp < 4 ? 3 * pgrp : 12 + 2 * (pgrp - 4);
  const int cksn = pgrp < 4 ? 3 : 2;
  auto pretile_fetch = [&]() {
    const rsrc_t r2 = mk_rsrc(a.w2, F_ * D_ * 2);
    const rsrc_t rcr = mk_rsrc(a.cre, D_ * P_ * 2), rci = mk_rsrc(a.cim, D_ * P_ * 2);
#pragma unroll
    for (int e = 0; e < 4; ++e) {
      const int pc = 4 * e + w;
      if (pc < 15) {
        const int q = pc * 64 + l, row = q / 10, ch = q - row * 10;
        glds16(r2, sm + K1_PT_W2 + pc * 1024, (96 * pgrp + row) * (D_ * 2) + pcg * 160 + ch * 16, 0);
      }
    }
#pragma unroll
    for (int e = 0; e < 4; ++e) {
      const int pc = 4 * e + w;
      if (pc < 5 * cksn) {
        const int ksl = pc / 5, nt = pc - 5 * ksl;
        const int vo = (80 * pcg + 16 * nt + r) * (P_ * 2) + (16 * (cks0 + ksl) + 8 * (g & 1)) * 2;
        if (g < 2) glds16(rcr, sm + K1_PT_C + pc * 1024, vo, 0);
        else glds16(rci, sm + K1_PT_C + pc * 1024, vo, 0);
      }
    }
  };
  u16 c_ldt[3], c_lre[3], c_lim[3];
  if (BU) {
#pragma unroll
    for (int c = 0; c < 3; ++c) {
      int p = 96 * jb + 16 * (3 * wn + c) + r;
      p = p < P_ ? p : P_ - 1;
      c_ldt[c] = a.ldt[p]; c_lre[c] = a.lre[p]; c_lim[c] = a.lim[p];
    }
  }
  SB();
#pragma unroll
  for (int s = 0; s < 3; ++s)
#pragma unroll
    for (int q = 0; q < 10; ++q) { stage_piece(s, q); if (q == 9) SB(); }

  f32x2 lam[3];
  if (BU) {
#pragma unroll
    for (int c = 0; c < 3; ++c) {
      const int p = 96 * jb + 16 * (3 * wn + c) + r;
      const bool ok = p < P_;
      const float dt = rbf(expf(bf2f(c_ldt[c])));
      const float lr = bf2f(c_lre[c]), li = bf2f(c_lim[c]);
      const float e = expf(dt * lr);
      float sn, cs;
      sincosf(dt * li, &sn, &cs);
      lam[c] = (f32x2){e * cs, e * sn};
      if (ti == 0 && wm == 0 && g == 0 && ok) {
        const float nr = lam[c][0] - 1.f, ni = lam[c][1], den = lr * lr + li * li;
        f32x4 cst = {lam[c][0], lam[c][1], (nr * lr + ni * li) / den, (ni * lr - nr * li) / den};
        *(f32x4*)(a.ws + WS_CONST + (size_t)p * 16) = cst;
      }
    }
  }
  if (blockIdx.x == 0 && tid < 32) *(unsigned*)(a.ws + WS_FLAG + tid * 128) = 0u;

  const int kf = wm ^ wn;
  int xo[2], wo[2][2];
#pragma unroll
  for (int h = 0; h < 2; ++h) {
    const int kk = kf ^ h;
    xo[h] = (64 * wm + r) * 128 + (((4 * kk + g) ^ (r & 7)) << 4);
    if (BU) {
      wo[h][0] = 16384 + (96 * wn + r) * 128 + (((4 * kk + g) ^ (r & 7)) << 4);
      wo[h][1] = 0;
    } else {
      const int q = r >> 2, p = r & 3, f = (q >> 1) | ((g & 1) << 1);
      const int base = 16384 + (4 * kk + g) * 3072 + q * 128 + 8 * p;
      wo[h][0] = base + (((2 * wn) ^ f) << 5);
      wo[h][1] = base + (((2 * wn + 1) ^ f) << 5);
    }
  }
  bf16x8 xf[2][4], wf[2][6];
  s16x4 wlo[2][6], whi[2][6];
  const unsigned lds0 = (unsigned)(uintptr_t)(LDS_AS char*)sm;
  auto rd_x = [&](int s, int h, int mt) {
    xf[h][mt] = *(const bf16x8*)(sm + (s % 3) * K1_SLOT + xo[h] + mt * 2048);
  };
  auto rd_w = [&](int s, int h, auto ntc) {
    constexpr int nt = decltype(ntc)::value;
    if (BU) wf[h][nt] = *(const bf16x8*)(sm + (s % 3) * K1_SLOT + wo[h][0] + nt * 2048);
    else {
      const unsigned b = lds0 + (s % 3) * K1_SLOT + wo[h][nt & 1];
      wlo[h][nt] = tr_asm<(nt >> 1) * 1024>(b);
      whi[h][nt] = tr_asm<(nt >> 1) * 1024 + 512>(b);
    }
  };
  auto rd_pair = [&](int s, int h, int n) {
    if (n < 2) { rd_x(s, h, 2 * n); rd_x(s, h, 2 * n + 1); }
    else if (n == 2) { rd_w(s, h, IC<0>{}); rd_w(s, h, IC<1>{}); }
    else if (n == 3) { rd_w(s, h, IC<2>{}); rd_w(s, h, IC<3>{}); }
    else { rd_w(s, h, IC<4>{}); rd_w(s, h, IC<5>{}); }
  };
  auto wait_frags = [&](int h) {
    if (BU) {
      asm volatile("s_waitcnt lgkmcnt(0)" ::: "memory");
    } else {
      asm volatile("s_waitcnt lgkmcnt(0)"
                   : "+v"(wlo[h][0]), "+v"(whi[h][0]), "+v"(wlo[h][1]), "+v"(whi[h][1]), "+v"(wlo[h][2]), "+v"(whi[h][2]),
                     "+v"(wlo[h][3]), "+v"(whi[h][3]), "+v"(wlo[h][4]), "+v"(whi[h][4]), "+v"(wlo[h][5]), "+v"(whi[h][5])
                   :: "memory");
#pragma unroll
      for (int n = 0; n < 6; ++n) wf[h][n] = __builtin_shufflevector(wlo[h][n], whi[h][n], 0, 1, 2, 3, 4, 5, 6, 7);
    }
  };

  f32x4 acc[6][4], sx[4], sq[4], cs[6];
  const f32x4 z4 = {0.f, 0.f, 0.f, 0.f};
#pragma unroll
  for (int n = 0; n < 6; ++n) {
    cs[n] = z4;
#pragma unroll
    for (int m = 0; m < 4; ++m) acc[n][m] = z4;
  }
#pragma unroll
  for (int m = 0; m < 4; ++m) { sx[m] = z4; sq[m] = z4; }
  const short one = 0x3f80;
  const bf16x8 ones = {one, one, one, one, one, one, one, one};

  auto mm = [&](int h, int n, int m) {
    if (BU) acc[n][m] = MFMA(xf[h][m], wf[h][n], acc[n][m]);
    else acc[n][m] = MFMA(wf[h][n], xf[h][m], acc[n][m]);
  };

  STAMP(a.ws, 0, 1);
  WAIT_VM(20);
  __builtin_amdgcn_s_barrier();
  SB();
  STAMP(a.ws, 0, 2);
#pragma unroll
  for (int n = 0; n < 5; ++n) rd_pair(0, 0, n);
  SB();

#pragma unroll
  for (int s = 0; s < 10; ++s) {
    wait_frags(0);
    SB();
#pragma unroll
    for (int n = 0; n < 6; ++n) {
      if (n < 5) rd_pair(s, 1, n);
#pragma unroll
      for (int m = 0; m < 4; ++m) mm(0, n, m);
      if (n < 4) {
        sx[n] = MFMA(xf[0][n], ones, sx[n]);
        if (BU) sq[n] = MFMA(xf[0][n], xf[0][n], sq[n]);
      }
      SB();
    }
    wait_frags(1);
    if (s < 9) {
      if (s < 8) WAIT_VM(10); else WAIT_VM(0);
      __builtin_amdgcn_s_barrier();
    }
    SB();
#pragma unroll
    for (int n = 0; n < 6; ++n) {
      if (s < 9 && n < 5) rd_pair(s + 1, 0, n);
      if (s + 3 < 10 && n < 5) { stage_piece(s + 3, 2 * n); stage_piece(s + 3, 2 * n + 1); }
      if (!BU && s == 8 && n == 0) pretile_fetch();
#pragma unroll
      for (int m = 0; m < 4; ++m) mm(1, n, m);
      cs[n] = MFMA(ones, wf[1][n], cs[n]);
      SB();
    }
  }

  STAMP(a.ws, 0, 3);
  float* SX = (float*)(sm + K1_SLOT);
  float* SQ = SX + 256;
  float* CS = SQ + 256;
  float* RM = CS + 384;
  float* RI = RM + 128;
#pragma unroll
  for (int m = 0; m < 4; ++m) {
    if (r == 0) *(f32x4*)(SX + wn * 128 + 64 * wm + 16 * m + 4 * g) = sx[m];
    if (BU) {
      const int e = r & 3;
      const float d = e == 0 ? sq[m][0] : e == 1 ? sq[m][1] : e == 2 ? sq[m][2] : sq[m][3];
      if (g == (r >> 2)) SQ[wn * 128 + 64 * wm + 16 * m + r] = d;
    }
  }
#pragma unroll
  for (int n = 0; n < 6; ++n) {
    const int col = BU ? (96 * wn + 16 * n + r) : (64 * (n >> 1) + 32 * wn + 16 * (n & 1) + r);
    if (g == 0) CS[wm * 192 + col] = cs[n][0];
  }
  __syncthreads();
  if (tid < 128) {
    const float mean = (SX[tid] + SX[128 + tid]) * (1.f / D_);
    const float mb = rbf(mean);
    if (BU) {
      const float var = fmaxf((SQ[tid] + SQ[128 + tid]) * (1.f / D_) - mean * mean, 0.f);
      const float sd = rbf(sqrtf(rbf(rbf(var) + 1e-5f)));
      const float ri = 1.f / sd;
      const int rr = tid & 15, mt = (tid >> 4) & 3;
      const int t = (tid & 64) + 16 * (rr >> 2) + 4 * mt + (rr & 3);
      RM[t] = mb * ri;
      RI[t] = ri;
      if (jb == 0) *(f32x2*)(a.ws + WS_STAT + (size_t)(ti * 128 + t) * 8) = (f32x2){mb, sd};
    } else {
      RM[tid] = mb;
    }
  }
  __syncthreads();

  STAMP(a.ws, 0, 4);
  const int rt = 2 * ti + wm;
  if (!BU) {
    float mu[4];
#pragma unroll
    for (int m = 0; m < 4; ++m) mu[m] = RM[64 * wm + 16 * m + r];
#pragma unroll
    for (int pc = 0; pc < 3; ++pc) {
      f32x4 c0 = *(const f32x4*)(CS + 64 * pc + 32 * wn + 4 * g) + *(const f32x4*)(CS + 192 + 64 * pc + 32 * wn + 4 * g);
      f32x4 c1 = *(const f32x4*)(CS + 64 * pc + 32 * wn + 16 + 4 * g) + *(const f32x4*)(CS + 192 + 64 * pc + 32 * wn + 16 + 4 * g);
      const int ks = 6 * tj + 2 * pc + wn;
#pragma unroll
      for (int m = 0; m < 4; ++m) {
        f32x4 v0 = acc[2 * pc][m] - c0 * mu[m], v1 = acc[2 * pc + 1][m] - c1 * mu[m];
        u32x4 o;
        o[0] = pk_bf16(fmaxf(v0[0], 0.f), fmaxf(v0[1], 0.f));
        o[1] = pk_bf16(fmaxf(v0[2], 0.f), fmaxf(v0[3], 0.f));
        o[2] = pk_bf16(fmaxf(v1[0], 0.f), fmaxf(v1[1], 0.f));
        o[3] = pk_bf16(fmaxf(v1[2], 0.f), fmaxf(v1[3], 0.f));
        *(u32x4*)(a.ws + WS_H1F + ((size_t)(rt * KSF + ks) * 4 + m) * 1024 + l * 16) = o;
      }
    }
#pragma unroll
    for (int e = 0; e < 4; ++e) {
      const int pc = 4 * e + w;
      if (pc < 15) {
        const int ksl = pc / 5, nt = pc - 5 * ksl;
        const char* b = sm + K1_PT_W2 + (32 * ksl + 4 * g + (r >> 2)) * 160 + nt * 32 + (r & 3) * 8;
        const bf16x8 v = tr_frag(b, 0, 16 * 160);
        *(bf16x8*)(a.ws + WS_W2F + ((size_t)(pcg * KSF + 3 * pgrp) * 5 + pc) * 1024 + l * 16) = v;
      }
    }
#pragma unroll
    for (int e = 0; e < 4; ++e) {
      const int pc = 4 * e + w;
      if (pc < 5 * cksn) {
        const u32x4 v = *(const u32x4*)(sm + K1_PT_C + pc * 1024 + l * 16);
        *(u32x4*)(a.ws + WS_CF + ((size_t)(pcg * KSY + cks0) * 5 + pc) * 1024 + l * 16) = v;
      }
    }
  } else {
    f32x4 rm[4], ri[4];
#pragma unroll
    for (int m = 0; m < 4; ++m) {
      rm[m] = *(const f32x4*)(RM + 64 * wm + 16 * g + 4 * m);
      ri[m] = *(const f32x4*)(RI + 64 * wm + 16 * g + 4 * m);
    }
#pragma unroll
    for (int c = 0; c < 3; ++c) {
      const int ctl = 3 * wn + c;
      const int ct = 6 * jb + ctl;
      const float csr = CS[32 * ctl + r] + CS[192 + 32 * ctl + r];
      const float csi = CS[32 * ctl + 16 + r] + CS[192 + 32 * ctl + 16 + r];
      const float la = lam[c][0], lb = lam[c][1];
      float hr = 0.f, hi = 0.f;
      float ore[16], oim[16];
#pragma unroll
      for (int m = 0; m < 4; ++m) {
        const f32x4 ur = acc[2 * c][m] * ri[m] - rm[m] * csr;
        const f32x4 ui = acc[2 * c + 1][m] * ri[m] - rm[m] * csi;
#pragma unroll
        for (int e = 0; e < 4; ++e) {
          const float nr = __builtin_fmaf(la, hr, __builtin_fmaf(-lb, hi, ur[e]));
          const float ni = __builtin_fmaf(la, hi, __builtin_fmaf(lb, hr, ui[e]));
          hr = nr; hi = ni;
          ore[4 * m + e] = hr; oim[4 * m + e] = hi;
        }
      }
      f32x2 l2 = cmul(lam[c], lam[c]), l4 = cmul(l2, l2), l8 = cmul(l4, l4), l16 = cmul(l8, l8), l32 = cmul(l16, l16);
      f32x2 v = {hr, hi};
      f32x2 u1 = {__shfl_up(v[0], 16), __shfl_up(v[1], 16)};
      if (g >= 1) v = cfma(l16, u1, v);
      f32x2 u2 = {__shfl_up(v[0], 32), __shfl_up(v[1], 32)};
      if (g >= 2) v = cfma(l32, u2, v);
      f32x2 ex = {__shfl_up(v[0], 16), __shfl_up(v[1], 16)};
      if (g == 0) ex = (f32x2){0.f, 0.f};
      if (ct < KSY) {
        char* hl = a.ws + WS_HLOC + (size_t)(rt * KSY + ct) * 4096 + l * 16;
#pragma unroll
        for (int q = 0; q < 2; ++q) {
          u32x4 o0, o1;
#pragma unroll
          for (int e = 0; e < 4; ++e) {
            o0[e] = pk_bf16(ore[8 * q + 2 * e], ore[8 * q + 2 * e + 1]);
            o1[e] = pk_bf16(oim[8 * q + 2 * e], oim[8 * q + 2 * e + 1]);
          }
          *(u32x4*)(hl + q * 1024) = o0;
          *(u32x4*)(hl + (2 + q) * 1024) = o1;
        }
        *(f32x2*)(a.ws + WS_PPRE + ((size_t)(rt * KSY + ct) * 64 + l) * 8) = ex;
        if (g == 3) *(f32x2*)(a.ws + WS_E64 + ((size_t)(ct * 32 + rt) * 16 + r) * 8) = v;
      }
    }
  }
}

__global__ __launch_bounds__(256, 1) void k1_kernel(K1Args a) {
  extern __shared__ __attribute__((aligned(16))) char sm[];
  const int b = blockIdx.x, x = b & 7, o = b >> 3;
  const int ti = (x & 1) * 8 + (o & 7);
  const int tj = (x >> 1) * 4 + (o >> 3);
  if (tj < 9) k1_body<false>(a, sm, ti, tj);
  else k1_body<true>(a, sm, ti, tj);
  STAMP(a.ws, 0, 5);
  STAMP(a.ws, 0, 6);
}

struct K2Args {
  const u16 *x, *dsk;
  char* ws;
  float* out;
};
#if defined(__HIP_DEVICE_COMPILE__)
#define NOPK __attribute__((target("no-packed-fp32-ops")))
#else
#define NOPK
#endif
#define DN __device__ __forceinline__ NOPK
constexpr int K2_RED_ROW = 336;
constexpr int K2_RED = 64 * K2_RED_ROW;
constexpr int K2_JOB = 4 * K2_RED;
constexpr int K2_LDS = K2_JOB + 4 * 4608;
#define PDV 3
constexpr int PD = PDV;
constexpr int NF = 14, NY = 10;

struct cpx { float re, im; };
DN cpx c_mul(cpx a, cpx b) { return {a.re * b.re - a.im * b.im, a.re * b.im + a.im * b.re}; }
DN cpx c_fma(cpx a, cpx b, cpx c) {
  return {__builtin_fmaf(a.re, b.re, __builtin_fmaf(-a.im, b.im, c.re)),
          __builtin_fmaf(a.re, b.im, __builtin_fmaf(a.im, b.re, c.im))};
}
DN cpx c_sel(bool p, cpx a, cpx b) { return {p ? a.re : b.re, p ? a.im : b.im}; }
DN float n_xor(float v, int l, int m) {
  return __builtin_bit_cast(float, __builtin_amdgcn_ds_bpermute((l ^ m) << 2, __builtin_bit_cast(int, v)));
}
DN unsigned n_pk(float lo, float hi) {
  f32x2 f = {lo, hi};
  return __builtin_bit_cast(unsigned, __builtin_convertvector(f, bf16x2));
}
DN float n_lo(unsigned u) { return __builtin_bit_cast(float, u << 16); }
DN float n_hi(unsigned u) { return __builtin_bit_cast(float, u & 0xffff0000u); }
DN float n_rbf(float f) { return n_lo(n_pk(f, 0.f)); }
DN rsrc_t n_rsrc(const void* p, unsigned bytes) {
  return __builtin_amdgcn_make_buffer_rsrc((void*)p, 0, bytes, 0x00020000);
}
DN void n_barrier() {
  asm volatile("s_waitcnt lgkmcnt(0)" ::: "memory");
  __builtin_amdgcn_s_barrier();
  asm volatile("" ::: "memory");
}

DN void k2_job(const K2Args& a, char* scr, int rt, int ct, int l) {
  const int c = l & 15, g = l >> 4;
  const char* hl = a.ws + WS_HLOC + (size_t)(rt * KSY + ct) * 4096 + l * 16;
  const f32x4 cst = *(const f32x4*)(a.ws + WS_CONST + (size_t)(16 * ct + c) * 16);
  f32x2 e[8];
#pragma unroll
  for (int i = 0; i < 8; ++i) e[i] = *(const f32x2*)(a.ws + WS_E64 + ((size_t)(ct * 32 + 4 * i + g) * 16 + c) * 8);
  const f32x2 pre = *(const f32x2*)(a.ws + WS_PPRE + ((size_t)(rt * KSY + ct) * 64 + l) * 8);
  u32x4 h[4];
#pragma unroll
  for (int q = 0; q < 4; ++q) h[q] = *(const u32x4*)(hl + q * 1024);
  SB();
  const cpx l1 = {cst[0], cst[1]};
  const cpx cf = {2.f * cst[2], 2.f * cst[3]};
  const cpx l2 = c_mul(l1, l1), l4 = c_mul(l2, l2), l8 = c_mul(l4, l4), l16 = c_mul(l8, l8), l32 = c_mul(l16, l16),
            l64 = c_mul(l32, l32), l128 = c_mul(l64, l64), l256 = c_mul(l128, l128);
  cpx H = {0.f, 0.f};
#pragma unroll
  for (int i = 0; i < 8; ++i) H = c_sel(4 * i + g < rt, c_fma(l256, H, cpx{e[i][0], e[i][1]}), H);
  const int ex = (rt - 1 - g) & 3;
  const cpx one = {1.f, 0.f};
  cpx cr = c_mul(c_mul(c_sel(ex & 1, l64, one), c_sel(ex & 2, l128, one)), H);
  cr.re += n_xor(cr.re, l, 16); cr.im += n_xor(cr.im, l, 16);
  cr.re += n_xor(cr.re, l, 32); cr.im += n_xor(cr.im, l, 32);
  const cpx lg = c_mul(c_sel(g & 1, l16, one), c_sel(g & 2, l32, one));
  const cpx carry = c_fma(lg, cr, cpx{pre[0], pre[1]});
  cpx G = c_mul(cf, c_mul(l1, carry));
  u32x4 ore[2], oim[2];
#pragma unroll
  for (int q = 0; q < 2; ++q)
#pragma unroll
    for (int i = 0; i < 4; ++i) {
      const cpx a0 = c_fma(cf, cpx{n_lo(h[q][i]), n_lo(h[2 + q][i])}, G);
      G = c_mul(l1, G);
      const cpx a1 = c_fma(cf, cpx{n_hi(h[q][i]), n_hi(h[2 + q][i])}, G);
      G = c_mul(l1, G);
      ore[q][i] = n_pk(a0.re, a1.re);
      oim[q][i] = n_pk(-a0.im, -a1.im);
    }
#pragma unroll
  for (int q = 0; q < 2; ++q) {
    *(u32x4*)(scr + c * 144 + g * 32 + q * 16) = ore[q];
    *(u32x4*)(scr + (16 + c) * 144 + g * 32 + q * 16) = oim[q];
  }
  const rsrc_t ro = n_rsrc(a.ws + WS_HF + (size_t)(rt * KSY + ct) * 4096, 4096);
  const char* b = scr + (8 * g + (c >> 2)) * 144 + (c & 3) * 8;
  u32x4 v[4];
#pragma unroll
  for (int mt = 0; mt < 4; ++mt) {
    s16x4 lo = __builtin_amdgcn_ds_read_tr16_b64_v4i16((LDS_AS s16x4*)(b + mt * 32));
    s16x4 hi = __builtin_amdgcn_ds_read_tr16_b64_v4i16((LDS_AS s16x4*)(b + mt * 32 + 4 * 144));
    v[mt] = __builtin_bit_cast(u32x4, __builtin_shufflevector(lo, hi, 0, 1, 2, 3, 4, 5, 6, 7));
  }
#pragma unroll
  for (int mt = 0; mt < 4; ++mt) __builtin_amdgcn_raw_buffer_store_b128(v[mt], ro, l * 16 + mt * 1024, 0, 16);
}

__global__ __launch_bounds__(512, 1) NOPK void k2_kernel(K2Args a) {
  extern __shared__ __attribute__((aligned(16))) char sm[];
  const int tid = threadIdx.x, l = tid & 63;
  const int w = __builtin_amdgcn_readfirstlane(tid >> 6);
  const int b = blockIdx.x, x = b & 7, o = b >> 3;
  const int rt = (x >> 1) * 8 + (o >> 2);
  const int cg = (x & 1) * 4 + (o & 3);
  unsigned* flag = (unsigned*)(a.ws + WS_FLAG + rt * 128);
  STAMP(a.ws, 1, 0);

  if (w < 4) {
    const int kf0 = w < 2 ? NF * w : 2 * NF + (NF - 1) * (w - 2);
    const rsrc_t raf = n_rsrc(a.ws + WS_H1F + (size_t)(rt * KSF + kf0) * 4096, (w < 2 ? NF : NF - 1) * 4096);
    const rsrc_t rbf_ = n_rsrc(a.ws + WS_W2F + (size_t)(cg * KSF + kf0) * 5120, (w < 2 ? NF : NF - 1) * 5120);
    const rsrc_t ray = n_rsrc(a.ws + WS_HF + (size_t)(rt * KSY + NY * w) * 4096, NY * 4096);
    const rsrc_t rby = n_rsrc(a.ws + WS_CF + (size_t)(cg * KSY + NY * w) * 5120, NY * 5120);
    f32x4 acc[5][4];
    bf16x8 fa[PD][4], fb[PD][5];
    const int vo = l * 16;
    auto ld_b = [&](int k, int j) NOPK {
      const int so = (k < NF ? k : k - NF) * 5120 + (j >> 2) * 4096;
      fb[k % PD][j] = __builtin_bit_cast(bf16x8, __builtin_amdgcn_raw_buffer_load_b128(k < NF ? rbf_ : rby, vo + (j & 3) * 1024, so, 0));
    };
    auto ld_a = [&](int k) NOPK {
#pragma unroll
      for (int i = 0; i < 4; ++i) {
        if (k < NF) fa[k % PD][i] = __builtin_bit_cast(bf16x8, __builtin_amdgcn_raw_buffer_load_b128(raf, vo + i * 1024, k * 4096, 0));
        else fa[k % PD][i] = __builtin_bit_cast(bf16x8, __builtin_amdgcn_raw_buffer_load_b128(ray, vo + i * 1024, (k - NF) * 4096, 16));
      }
    };
    char* red = sm + w * K2_RED + (l & 15) * K2_RED_ROW + (l >> 4) * 16;
    auto dump = [&]() NOPK {
#pragma unroll
      for (int j = 0; j < 5; ++j)
#pragma unroll
        for (int i = 0; i < 4; ++i) *(f32x4*)(red + i * 16 * K2_RED_ROW + j * 64) = acc[j][i];
    };
#pragma unroll
    for (int k = 0; k < PD; ++k) {
      ld_a(k);
#pragma unroll
      for (int j = 0; j < 5; ++j) ld_b(k, j);
    }
    SB();
    unsigned seen = 0;
    const f32x4 z4 = {0.f, 0.f, 0.f, 0.f};
#pragma unroll
    for (int k = 0; k < NF + NY; ++k) {
      const bool first = (k == 0 || k == NF);
#pragma unroll
      for (int j = 0; j < 5; ++j) {
#pragma unroll
        for (int i = 0; i < 4; ++i)
          acc[j][i] = MFMA(fb[k % PD][j], fa[k % PD][i], first ? z4 : acc[j][i]);
        SB();
        if (k + PD < NF + NY) ld_b(k + PD, j);
        SB();
      }
      if (k + PD == NF - 2) seen = __hip_atomic_load(flag, __ATOMIC_RELAXED, __HIP_MEMORY_SCOPE_AGENT);
      if (k + PD == NF) {
        asm volatile("" : "+v"(seen));
        unsigned spins = 0;
        while (seen < (unsigned)KSY && spins < (1u << 22)) {
          __builtin_amdgcn_s_sleep(2);
          seen = __hip_atomic_load(flag, __ATOMIC_RELAXED, __HIP_MEMORY_SCOPE_AGENT);
          ++spins;
        }
        __builtin_amdgcn_fence(__ATOMIC_ACQUIRE, "wavefront");
        STAMP(a.ws, 1, 2);
      }
      if (k + PD < NF + NY) ld_a(k + PD);
      SB();
      if (k == NF - 1) {
        dump();
        n_barrier();
        STAMP(a.ws, 1, 1);
        SB();
      }
    }
    STAMP(a.ws, 1, 3);
    n_barrier();
    dump();
    n_barrier();
    STAMP(a.ws, 1, 4);
  } else {
    __builtin_amdgcn_s_setprio(2);
    const int hw = w - 4, ht = tid - 256;
    k2_job(a, sm + K2_JOB + hw * 4608, rt, 5 * cg + hw, l);
    if (hw == 0) k2_job(a, sm + K2_JOB, rt, 5 * cg + 4, l);
    asm volatile("s_waitcnt vmcnt(0)" ::: "memory");
    if (l == 0) __hip_atomic_fetch_add(flag, hw == 0 ? 2u : 1u, __ATOMIC_RELAXED, __HIP_MEMORY_SCOPE_AGENT);
    STAMP(a.ws, 1, 1);
    float xs[5][4], xd[5][4], gt[5][4], ri[5];
    f32x2 st[5];
    uint2 xv[5], dv[5];
#pragma unroll
    for (int e = 0; e < 5; ++e) {
      const int idx = ht + 256 * e, row = idx / 20, c4 = idx - row * 20;
      const int t = rt * 64 + row, d = cg * 80 + c4 * 4;
      st[e] = *(const f32x2*)(a.ws + WS_STAT + (size_t)t * 8);
      xv[e] = *(const uint2*)(a.x + (size_t)t * D_ + d);
      dv[e] = *(const uint2*)(a.dsk + d);
    }
    SB();
#pragma unroll
    for (int e = 0; e < 5; ++e) {
      xs[e][0] = n_lo(xv[e].x); xs[e][1] = n_hi(xv[e].x); xs[e][2] = n_lo(xv[e].y); xs[e][3] = n_hi(xv[e].y);
      const float ds[4] = {n_lo(dv[e].x), n_hi(dv[e].x), n_lo(dv[e].y), n_hi(dv[e].y)};
      ri[e] = 1.f / st[e][1];
#pragma unroll
      for (int i = 0; i < 4; ++i) xd[e][i] = n_rbf(n_rbf(n_rbf(xs[e][i] - st[e][0]) / st[e][1]) * ds[i]);
    }
    SB();
    STAMP(a.ws, 1, 2);
    n_barrier();
    STAMP(a.ws, 1, 3);
#pragma unroll
    for (int e = 0; e < 5; ++e) {
      const int idx = ht + 256 * e, row = idx / 20, c4 = idx - row * 20;
      const char* p = sm + row * K2_RED_ROW + c4 * 16;
      const f32x4 p0 = *(const f32x4*)p, p1 = *(const f32x4*)(p + K2_RED), p2 = *(const f32x4*)(p + 2 * K2_RED),
                  p3 = *(const f32x4*)(p + 3 * K2_RED);
#pragma unroll
      for (int i = 0; i < 4; ++i) {
        const float fv = n_rbf(((p0[i] + p1[i]) + (p2[i] + p3[i])) * ri[e]);
        gt[e][i] = n_rbf(fv / (1.f + __builtin_amdgcn_exp2f(-1.44269504088896f * fv)));
      }
    }
#pragma unroll
    for (int e = 0; e < 5; ++e)
      asm volatile("" : "+v"(gt[e][0]), "+v"(gt[e][1]), "+v"(gt[e][2]), "+v"(gt[e][3]), "+v"(xd[e][0]), "+v"(xd[e][1]),
                        "+v"(xd[e][2]), "+v"(xd[e][3]));
    n_barrier();
    n_barrier();
    STAMP(a.ws, 1, 4);
#pragma unroll
    for (int e = 0; e < 5; ++e) {
      const int idx = ht + 256 * e, row = idx / 20, c4 = idx - row * 20;
      const char* p = sm + row * K2_RED_ROW + c4 * 16;
      const f32x4 p0 = *(const f32x4*)p, p1 = *(const f32x4*)(p + K2_RED), p2 = *(const f32x4*)(p + 2 * K2_RED),
                  p3 = *(const f32x4*)(p + 3 * K2_RED);
      f32x4 o4;
#pragma unroll
      for (int i = 0; i < 4; ++i) o4[i] = xs[e][i] + (((p0[i] + p1[i]) + (p2[i] + p3[i])) + xd[e][i]) * gt[e][i];
      *(f32x4*)(a.out + (size_t)(rt * 64 + row) * D_ + cg * 80 + c4 * 4) = o4;
    }
  }
  STAMP(a.ws, 1, 5);
  STAMP(a.ws, 1, 6);
}

extern "C" void kernel_launch(void* const* d_in, const int* in_sizes, int n_in, void* d_out, int out_size,
                              void* d_ws, size_t ws_size, hipStream_t stream) {
  (void)in_sizes; (void)n_in; (void)out_size; (void)ws_size;
  K1Args a1;
  a1.x = (const u16*)d_in[0]; a1.lre = (const u16*)d_in[1]; a1.lim = (const u16*)d_in[2];
  a1.bre = (const u16*)d_in[3]; a1.bim = (const u16*)d_in[4]; a1.cre = (const u16*)d_in[5];
  a1.cim = (const u16*)d_in[6]; a1.ldt = (const u16*)d_in[7]; a1.w1 = (const u16*)d_in[10];
  a1.w2 = (const u16*)d_in[11]; a1.ws = (char*)d_ws;
  K2Args a2;
  a2.x = (const u16*)d_in[0]; a2.dsk = (const u16*)d_in[8]; a2.ws = (char*)d_ws; a2.out = (float*)d_out;
  (void)hipFuncSetAttribute((const void*)k1_kernel, hipFuncAttributeMaxDynamicSharedMemorySize, K1_LDS);
  (void)hipFuncSetAttribute((const void*)k2_kernel, hipFuncAttributeMaxDynamicSharedMemorySize, K2_LDS);
  k1_kernel<<<256, 256, K1_LDS, stream>>>(a1);
  k2_kernel<<<256, 512, K2_LDS, stream>>>(a2);
}
```

```cpp
#include <hip/hip_runtime.h>
#include <stdint.h>

typedef unsigned short u16;
typedef unsigned long long u64;
typedef __attribute__((ext_vector_type(8))) short bf16x8;
typedef __attribute__((ext_vector_type(4))) short s16x4;
typedef __attribute__((ext_vector_type(4))) float f32x4;
typedef __attribute__((ext_vector_type(2))) float f32x2;
typedef __attribute__((ext_vector_type(4))) unsigned u32x4;
typedef __attribute__((ext_vector_type(2))) unsigned u32x2;
typedef __attribute__((ext_vector_type(2))) __bf16 bf16x2;
typedef __amdgpu_buffer_rsrc_t rsrc_t;
#define LDS_AS __attribute__((address_space(3)))
#if defined(__HIP_DEVICE_COMPILE__)
#define NOPK __attribute__((target("no-packed-fp32-ops")))
#else
#define NOPK
#endif
#define DN __device__ __forceinline__ NOPK
#define MFMA(a, b, c) __builtin_amdgcn_mfma_f32_16x16x32_bf16((a), (b), (c), 0, 0, 0)
#define SB() __builtin_amdgcn_sched_barrier(0)
#define WAIT_VM(n) asm volatile("s_waitcnt vmcnt(" #n ")" ::: "memory")
#define SC1 16
template <int V> struct IC { static constexpr int value = V; };

constexpr int L_ = 2048, D_ = 640, P_ = 640, F_ = 1728;
constexpr int KSF = F_ / 32;
constexpr int KSY = P_ / 16;

constexpr unsigned WS_H1F = 0;
constexpr unsigned WS_HLOC = WS_H1F + 32u * KSF * 4096;
constexpr unsigned WS_HF = WS_HLOC + 32u * KSY * 4096;
constexpr unsigned WS_W2F = WS_HF + 32u * KSY * 4096;
constexpr unsigned WS_CF = WS_W2F + 8u * KSF * 5120;
constexpr unsigned WS_PPRE = WS_CF + 8u * KSY * 5120;
constexpr unsigned WS_E64 = WS_PPRE + 32u * KSY * 512;
constexpr unsigned WS_STAT = WS_E64 + KSY * 32u * 128;
constexpr unsigned WS_CSF = WS_STAT + L_ * 8u;
constexpr unsigned WS_CSS = WS_CSF + 9u * 16 * 768;
constexpr unsigned WS_FST = WS_CSS + 7u * 768 + 768;
constexpr unsigned WS_FCS = WS_FST + 2048;
constexpr unsigned WS_FPT = WS_FCS + 2048;
constexpr unsigned WS_FAF = WS_FPT + 4096;
constexpr unsigned WS_FAS = WS_FAF + 8192;
constexpr unsigned WS_FH = WS_FAS + 4096;
constexpr unsigned WS_EP = WS_FH + 8192;
constexpr unsigned WS_END = WS_EP + 128;

constexpr int SLOT = 40960;
constexpr int LD_AUX = 3 * SLOT;
constexpr int LD_RM = LD_AUX + 18432;
constexpr int LD_RI = LD_RM + 512;
constexpr int LD_CS = LD_RI + 512;
constexpr int LD_LAM = LD_CS + 768;
constexpr int LD_SYNC = LD_LAM + 768;
constexpr int LDS_BYTES = LD_SYNC + 128;
constexpr int SY_TAG = LD_SYNC, SY_FULL = LD_SYNC + 16, SY_FREE = LD_SYNC + 32, SY_GATH = LD_SYNC + 48,
              SY_PTOK = LD_SYNC + 56, SY_P2GO = LD_SYNC + 64, SY_YRDY = LD_SYNC + 80;
constexpr int WREG = 30720;
constexpr int RED_ROW = 336;
constexpr int PD = 3, NF = 14, NY = 10;
constexpr unsigned SPIN_MAX = 1u << 18;

#define STAMP(i)

struct Args {
  const u16 *x, *lre, *lim, *bre, *bim, *cre, *cim, *ldt, *dsk, *w1, *w2;
  char* ws;
  float* out;
};

struct cpx { float re, im; };
DN cpx c_mul(cpx a, cpx b) { return {a.re * b.re - a.im * b.im, a.re * b.im + a.im * b.re}; }
DN cpx c_fma(cpx a, cpx b, cpx c) {
  return {__builtin_fmaf(a.re, b.re, __builtin_fmaf(-a.im, b.im, c.re)),
          __builtin_fmaf(a.re, b.im, __builtin_fmaf(a.im, b.re, c.im))};
}
DN cpx c_sel(bool p, cpx a, cpx b) { return {p ? a.re : b.re, p ? a.im : b.im}; }
DN float lane_get(float v, int src) {
  return __builtin_bit_cast(float, __builtin_amdgcn_ds_bpermute(src << 2, __builtin_bit_cast(int, v)));
}
DN unsigned pk_bf16(float lo, float hi) {
  f32x2 f = {lo, hi};
  return __builtin_bit_cast(unsigned, __builtin_convertvector(f, bf16x2));
}
DN float as_f(unsigned u) { return __builtin_bit_cast(float, u); }
DN unsigned as_u(float f) { return __builtin_bit_cast(unsigned, f); }
DN float lo_f(unsigned u) { return __builtin_bit_cast(float, u << 16); }
DN float hi_f(unsigned u) { return __builtin_bit_cast(float, u & 0xffff0000u); }
DN float bf2f(u16 v) { return lo_f(v); }
DN float rbf(float f) { return lo_f(pk_bf16(f, 0.f)); }
DN rsrc_t mk_rsrc(const void* p, unsigned bytes) {
  return __builtin_amdgcn_make_buffer_rsrc((void*)p, 0, bytes, 0x00020000);
}
template <int AUX>
DN void glds16(rsrc_t r, char* lds, int voff, int soff) {
  __builtin_amdgcn_raw_ptr_buffer_load_lds(r, (LDS_AS void*)lds, 16, voff, soff, 0, AUX);
}
#define BARS(i) bar()
DN void glds16_g(const void* src, char* lds, bool on) {
  if (on) __builtin_amdgcn_global_load_lds((const __attribute__((address_space(1))) void*)src, (LDS_AS void*)lds, 16, 0, 0);
}
DN void bar() {
  asm volatile("s_waitcnt lgkmcnt(0)" ::: "memory");
  __builtin_amdgcn_s_barrier();
  asm volatile("" ::: "memory");
  SB();
}
template <class T> DN T lds_ld(const char* p) { return *(volatile LDS_AS T*)(LDS_AS char*)p; }
DN void lds_st(char* p, unsigned v) {
  asm volatile("" ::: "memory");
  *(volatile LDS_AS unsigned*)(LDS_AS char*)p = v;
  asm volatile("" ::: "memory");
}
DN unsigned vmin(u32x2 v) { return v[0] < v[1] ? v[0] : v[1]; }
DN unsigned vmin(u32x4 v) {
  const unsigned a = v[0] < v[1] ? v[0] : v[1], b = v[2] < v[3] ? v[2] : v[3];
  return a < b ? a : b;
}
DN unsigned vmin(unsigned v) { return v; }
template <class T> DN void lds_wait(const char* p, unsigned n, T first) {
  unsigned spins = 0;
  while (__builtin_amdgcn_readfirstlane(vmin(first)) < n && spins < 64u * SPIN_MAX) {
    __builtin_amdgcn_s_sleep(1);
    first = lds_ld<T>(p);
    ++spins;
  }
  asm volatile("" ::: "memory");
}
template <int N, int OFF2>
DN void lds_wait2(unsigned addr, unsigned f0, unsigned f1) {
  unsigned s0, s1, cnt;
  asm volatile(
      "s_mov_b32 %4, 0\n"
      "1:\n\t"
      "v_readfirstlane_b32 %2, %0\n\t"
      "v_readfirstlane_b32 %3, %1\n\t"
      "s_add_u32 %4, %4, 1\n\t"
      "s_min_u32 %2, %2, %3\n\t"
      "s_cmp_ge_u32 %2, %6\n\t"
      "s_cbranch_scc1 2f\n\t"
      "s_bitcmp1_b32 %4, 24\n\t"
      "s_cbranch_scc1 2f\n\t"
      "s_sleep 1\n\t"
      "ds_read_b32 %0, %5\n\t"
      "ds_read_b32 %1, %5 offset:%7\n\t"
      "s_waitcnt lgkmcnt(0)\n\t"
      "s_branch 1b\n"
      "2:"
      : "+v"(f0), "+v"(f1), "=&s"(s0), "=&s"(s1), "=&s"(cnt)
      : "v"(addr), "i"(N), "i"(OFF2)
      : "scc", "memory");
}
DN unsigned lds_addr(const char* p) { return (unsigned)(uintptr_t)(LDS_AS const char*)p; }
DN bf16x8 tr_frag(const char* p0, const char* p1) {
  s16x4 lo = __builtin_amdgcn_ds_read_tr16_b64_v4i16((LDS_AS s16x4*)p0);
  s16x4 hi = __builtin_amdgcn_ds_read_tr16_b64_v4i16((LDS_AS s16x4*)p1);
  return __builtin_shufflevector(lo, hi, 0, 1, 2, 3, 4, 5, 6, 7);
}
DN u64 flag_ld(const char* p) { return __hip_atomic_load((const u64*)p, __ATOMIC_RELAXED, __HIP_MEMORY_SCOPE_AGENT); }
DN void flag_st(char* p, u64 v) { __hip_atomic_store((u64*)p, v, __ATOMIC_RELAXED, __HIP_MEMORY_SCOPE_AGENT); }
DN void flag_wait(const char* p, bool need, u64 tag, u64 first) {
  unsigned spins = 0;
  while (__builtin_amdgcn_ballot_w64(need && first != tag) != 0 && spins < SPIN_MAX) {
    __builtin_amdgcn_s_sleep(1);
    first = flag_ld(p);
    ++spins;
  }
  __builtin_amdgcn_fence(__ATOMIC_ACQUIRE, "wavefront");
}
DN float exp_acc(float x) {
  const float l2e = 1.44269502162933349609375f, l2e_lo = 1.925963033500011e-08f;
  const float t = x * l2e;
  const float e = __builtin_fmaf(x, l2e_lo, __builtin_fmaf(x, l2e, -t));
  const float r = __builtin_amdgcn_exp2f(t);
  return __builtin_fmaf(r, e * 0.693147180559945f, r);
}
DN void sincos_acc(float x, float& s, float& c) {
  const float k = __builtin_rintf(x * 0.636619772367581f);
  float r = __builtin_fmaf(k, -1.57079601287841796875f, x);
  r = __builtin_fmaf(k, -3.1391647326017846353352069854736328125e-7f, r);
  r = __builtin_fmaf(k, -5.390302529957764765544681040410068817436695098876953125e-15f, r);
  const float z = r * r;
  float ps = __builtin_fmaf(z, 2.7183114939898219064e-6f, -1.98393348360966317347e-4f);
  ps = __builtin_fmaf(z, ps, 8.3333293858894631756e-3f);
  ps = __builtin_fmaf(z, ps, -1.66666666416265235595e-1f);
  const float sn = __builtin_fmaf(r * z, ps, r);
  float pc = __builtin_fmaf(z, 2.43904487962774090654e-5f, -1.38867637746099294692e-3f);
  pc = __builtin_fmaf(z, pc, 4.16666233237390631894e-2f);
  pc = __builtin_fmaf(z, pc, -4.99999997251031003120e-1f);
  const float cs = __builtin_fmaf(z, pc, 1.f);
  const int q = (int)k;
  const float s0 = (q & 1) ? cs : sn, c0 = (q & 1) ? sn : cs;
  s = (q & 2) ? -s0 : s0;
  c = ((q + 1) & 2) ? -c0 : c0;
}
DN void chan_consts(u16 ldt, u16 lre, u16 lim, cpx& lam, cpx& coef) {
  const float dt = rbf(exp_acc(bf2f(ldt)));
  const float lr = bf2f(lre), li = bf2f(lim);
  const float e = exp_acc(dt * lr);
  float sn, cs;
  sincos_acc(dt * li, sn, cs);
  lam = {e * cs, e * sn};
  const float nr = lam.re - 1.f, ni = lam.im, den = lr * lr + li * li;
  coef = {(nr * lr + ni * li) / den, (ni * lr - nr * li) / den};
}
DN float sum8(u32x4 v) {
  return ((lo_f(v[0]) + hi_f(v[0])) + (lo_f(v[1]) + hi_f(v[1]))) + ((lo_f(v[2]) + hi_f(v[2])) + (lo_f(v[3]) + hi_f(v[3])));
}
DN float red8(float v, int l) {
  v += lane_get(v, l ^ 1);
  v += lane_get(v, l ^ 2);
  v += lane_get(v, l ^ 4);
  return v;
}
DN int kf_start(int w) { return w < 2 ? NF * w : 2 * NF + (NF - 1) * (w - 2); }

template <bool BU>
DN void p1_gemm(const Args& a, char* sm, int ti, int tj, int w, int l) {
  const int wm = w >> 1, wn = w & 1, r = l & 15, g = l >> 4, jb = tj - 9;
  int xo[2], wo[2][2];
#pragma unroll
  for (int kk = 0; kk < 2; ++kk) {
    xo[kk] = (64 * wm + r) * 128 + (((4 * kk + g) ^ (r & 7)) << 4);
    if (BU) {
      wo[kk][0] = 16384 + (96 * wn + r) * 128 + (((4 * kk + g) ^ (r & 7)) << 4);
      wo[kk][1] = 0;
    } else {
      const int q = r >> 2, p = r & 3, f = (q >> 1) | ((g & 1) << 1);
      const int base = 16384 + (4 * kk + g) * 3072 + q * 128 + 8 * p;
      wo[kk][0] = base + (((2 * wn) ^ f) << 5);
      wo[kk][1] = base + (((2 * wn + 1) ^ f) << 5);
    }
  }
  bf16x8 xf[2][4], wf[2][6];
  auto rd = [&](int s, int h, int i) NOPK {
    const char* slot = sm + (s % 3) * SLOT;
    if (i >= 1 && i <= 4) {
      xf[h][i - 1] = *(const bf16x8*)(slot + xo[h] + (i - 1) * 2048);
    } else {
      const int nt = i == 0 ? 0 : i - 4;
      if (BU) wf[h][nt] = *(const bf16x8*)(slot + wo[h][0] + nt * 2048);
      else {
        const char* b = slot + wo[h][nt & 1] + (nt >> 1) * 1024;
        wf[h][nt] = tr_frag(b, b + 512);
      }
    }
  };
  f32x4 acc[6][4];
  const f32x4 z4 = {0.f, 0.f, 0.f, 0.f};
  u32x2 fl = {0u, 0u};
  auto half = [&](int h, bool first, bool pre, int ps, int ph) NOPK {
#pragma unroll
    for (int n = 0; n < 6; ++n)
#pragma unroll
      for (int m = 0; m < 4; ++m) {
        const int idx = 4 * n + m;
        if (pre && idx < 10) rd(ps, ph, idx);
        if (h == 0 && idx == 10) lds_st(sm + SY_FREE + 4 * w, ps + 1);
        if (h == 0 && idx == 16) fl = lds_ld<u32x2>(sm + (ps < 9 ? SY_FULL : SY_GATH));
        if (BU) acc[n][m] = MFMA(xf[h][m], wf[h][n], first ? z4 : acc[n][m]);
        else acc[n][m] = MFMA(wf[h][n], xf[h][m], first ? z4 : acc[n][m]);
        if (idx < 12 || m == 3 || idx == 16) SB();
      }
  };
  bar();
  lds_wait2<1, 4>(lds_addr(sm + SY_FULL), 0u, 0u);
  STAMP(1);
#pragma unroll
  for (int i = 0; i < 10; ++i) rd(0, 0, i);
  SB();
  auto stage = [&](auto sc) NOPK {
    constexpr int s = decltype(sc)::value;
    half(0, s == 0, true, s, 1);
    lds_wait2<(s < 9 ? s + 2 : 1), 4>(lds_addr(sm + (s < 9 ? SY_FULL : SY_GATH)), fl[0], fl[1]);
    SB();
    half(1, false, s < 9, s + 1, 0);
  };
  stage(IC<0>{}); stage(IC<1>{}); stage(IC<2>{}); stage(IC<3>{}); stage(IC<4>{});
  stage(IC<5>{}); stage(IC<6>{}); stage(IC<7>{}); stage(IC<8>{}); stage(IC<9>{});
  STAMP(2);

  const u64 tag = *(const u64*)(sm + SY_TAG);
  const float* RM = (const float*)(sm + LD_RM);
  const float* RI = (const float*)(sm + LD_RI);
  const float* CS = (const float*)(sm + LD_CS);
  const int rt = 2 * ti + wm;
  const rsrc_t rws = mk_rsrc(a.ws, WS_END);
  if (!BU) {
    float mu[4];
#pragma unroll
    for (int m = 0; m < 4; ++m) mu[m] = RM[64 * wm + 16 * m + r];
#pragma unroll
    for (int pc = 0; pc < 3; ++pc) {
      const f32x4 c0 = *(const f32x4*)(CS + 64 * pc + 32 * wn + 4 * g);
      const f32x4 c1 = *(const f32x4*)(CS + 64 * pc + 32 * wn + 16 + 4 * g);
      const int ks = 6 * tj + 2 * pc + wn;
#pragma unroll
      for (int m = 0; m < 4; ++m) {
        float v[8];
#pragma unroll
        for (int e = 0; e < 4; ++e) {
          v[e] = __builtin_fmaxf(__builtin_fmaf(-c0[e], mu[m], acc[2 * pc][m][e]), 0.f);
          v[4 + e] = __builtin_fmaxf(__builtin_fmaf(-c1[e], mu[m], acc[2 * pc + 1][m][e]), 0.f);
        }
        const u32x4 o = {pk_bf16(v[0], v[1]), pk_bf16(v[2], v[3]), pk_bf16(v[4], v[5]), pk_bf16(v[6], v[7])};
        __builtin_amdgcn_raw_buffer_store_b128(o, rws, WS_H1F + ((rt * KSF + ks) * 4 + m) * 1024 + l * 16, 0, SC1);
      }
    }
    WAIT_VM(0);
    if (l == 0) flag_st(a.ws + WS_FAF + (rt * 32 + 2 * tj + wn) * 8, tag);
  } else {
    f32x4 rm[4], ri[4];
#pragma unroll
    for (int m = 0; m < 4; ++m) {
      rm[m] = *(const f32x4*)(RM + 64 * wm + 16 * g + 4 * m);
      ri[m] = *(const f32x4*)(RI + 64 * wm + 16 * g + 4 * m);
    }
    cpx lam[3], h[3];
    float csr[3], csi[3];
#pragma unroll
    for (int c = 0; c < 3; ++c) {
      const int ctl = 3 * wn + c;
      const f32x2 lv = *(const f32x2*)(sm + LD_LAM + (16 * ctl + r) * 8);
      lam[c] = {lv[0], lv[1]};
      csr[c] = CS[32 * ctl + r];
      csi[c] = CS[32 * ctl + 16 + r];
      h[c] = {0.f, 0.f};
    }
#pragma unroll
    for (int m = 0; m < 4; ++m)
#pragma unroll
      for (int e = 0; e < 4; ++e)
#pragma unroll
        for (int c = 0; c < 3; ++c) {
          const float ur = __builtin_fmaf(acc[2 * c][m][e], ri[m][e], -rm[m][e] * csr[c]);
          const float ui = __builtin_fmaf(acc[2 * c + 1][m][e], ri[m][e], -rm[m][e] * csi[c]);
          h[c] = c_fma(lam[c], h[c], cpx{ur, ui});
          acc[2 * c][m][e] = h[c].re;
          acc[2 * c + 1][m][e] = h[c].im;
        }
    const int nct = (jb == 6 && wn == 1) ? 1 : 3;
#pragma unroll
    for (int c = 0; c < 3; ++c) {
      const int ct = 6 * jb + 3 * wn + c;
      if (c < nct) {
#pragma unroll
        for (int q = 0; q < 4; ++q) {
          const f32x4 v0 = acc[2 * c + (q >> 1)][2 * (q & 1)], v1 = acc[2 * c + (q >> 1)][2 * (q & 1) + 1];
          const u32x4 o = {pk_bf16(v0[0], v0[1]), pk_bf16(v0[2], v0[3]), pk_bf16(v1[0], v1[1]), pk_bf16(v1[2], v1[3])};
          __builtin_amdgcn_raw_buffer_store_b128(o, rws, WS_HLOC + ((rt * KSY + ct) * 4 + q) * 1024 + l * 16, 0, SC1);
        }
      }
    }
#pragma unroll
    for (int c = 0; c < 3; ++c) {
      const int ct = 6 * jb + 3 * wn + c;
      const cpx l2 = c_mul(lam[c], lam[c]), l4 = c_mul(l2, l2), l8 = c_mul(l4, l4), l16 = c_mul(l8, l8), l32 = c_mul(l16, l16);
      cpx v = h[c];
      const cpx u1 = {lane_get(v.re, l - 16), lane_get(v.im, l - 16)};
      v = c_sel(g >= 1, c_fma(l16, u1, v), v);
      const cpx u2 = {lane_get(v.re, l - 32), lane_get(v.im, l - 32)};
      v = c_sel(g >= 2, c_fma(l32, u2, v), v);
      cpx ex = {lane_get(v.re, l - 16), lane_get(v.im, l - 16)};
      ex = c_sel(g == 0, cpx{0.f, 0.f}, ex);
      if (c < nct) {
        const u32x2 pv = {as_u(ex.re), as_u(ex.im)};
        __builtin_amdgcn_raw_buffer_store_b64(pv, rws, WS_PPRE + ((rt * KSY + ct) * 64 + l) * 8, 0, SC1);
        const u32x2 ev = {as_u(v.re), as_u(v.im)};
        if (g == 3) __builtin_amdgcn_raw_buffer_store_b64(ev, rws, WS_E64 + ((ct * 32 + rt) * 16 + r) * 8, 0, SC1);
      }
    }
    WAIT_VM(0);
    if (l == 0) flag_st(a.ws + WS_FAS + ((2 * jb + wn) * 32 + rt) * 8, tag);
  }
  STAMP(3);
}

DN void p2_gemm(const Args& a, char* sm, int rt, int cg, int w, int l) {
  const int kf0 = kf_start(w), nf = w < 2 ? NF : NF - 1;
  const rsrc_t raf = mk_rsrc(a.ws + WS_H1F + (rt * KSF + kf0) * 4096, nf * 4096);
  const rsrc_t rbf_ = mk_rsrc(a.ws + WS_W2F + (cg * KSF + kf0) * 5120, nf * 5120);
  const rsrc_t ray = mk_rsrc(a.ws + WS_HF + (rt * KSY + NY * w) * 4096, NY * 4096);
  const rsrc_t rby = mk_rsrc(a.ws + WS_CF + (cg * KSY + NY * w) * 5120, NY * 5120);
  f32x4 acc[5][4];
  bf16x8 fa[PD][4], fb[PD][5];
  const int vo = l * 16;
  char* wreg = sm + w * WREG;
  auto from_lds = [](int k) NOPK { return k < 12 && (k & 1); };
  auto ld_b = [&](int k, int j) NOPK {
    if (from_lds(k)) {
      fb[k % PD][j] = *(const bf16x8*)(wreg + (k >> 1) * 5120 + j * 1024 + vo);
    } else {
      const int so = (k < NF ? k : k - NF) * 5120 + (j >> 2) * 4096;
      fb[k % PD][j] = __builtin_bit_cast(
          bf16x8, __builtin_amdgcn_raw_buffer_load_b128(k < NF ? rbf_ : rby, vo + (j & 3) * 1024, so, SC1));
    }
  };
  auto ld_a = [&](int k) NOPK {
#pragma unroll
    for (int i = 0; i < 4; ++i)
      fa[k % PD][i] = __builtin_bit_cast(
          bf16x8, __builtin_amdgcn_raw_buffer_load_b128(k < NF ? raf : ray, vo + i * 1024, (k < NF ? k : k - NF) * 4096, SC1));
  };
  char* red = wreg + (l & 15) * RED_ROW + (l >> 4) * 16;
  auto dump = [&]() NOPK {
#pragma unroll
    for (int j = 0; j < 5; ++j)
#pragma unroll
      for (int i = 0; i < 4; ++i) *(f32x4*)(red + i * 16 * RED_ROW + j * 64) = acc[j][i];
  };
  lds_wait2<1, 0>(lds_addr(sm + SY_PTOK), 0u, 0u);
#pragma unroll
  for (int k = 0; k < PD; ++k)
    if (!from_lds(k)) {
#pragma unroll
      for (int j = 0; j < 5; ++j) ld_b(k, j);
    }
  SB();
  lds_wait2<1, 4>(lds_addr(sm + SY_P2GO), 0u, 0u);
  lds_wait2<1, 0>(lds_addr(sm + SY_P2GO + 8), 0u, 0u);
  SB();
  STAMP(4);
#pragma unroll
  for (int k = 0; k < PD; ++k) {
    ld_a(k);
    if (from_lds(k)) {
#pragma unroll
      for (int j = 0; j < 5; ++j) ld_b(k, j);
    }
  }
  SB();
  const f32x4 z4 = {0.f, 0.f, 0.f, 0.f};
  unsigned yr = 0u;
#pragma unroll
  for (int k = 0; k < NF + NY; ++k) {
    const bool first = (k == 0 || k == NF);
#pragma unroll
    for (int j = 0; j < 5; ++j) {
#pragma unroll
      for (int i = 0; i < 4; ++i) acc[j][i] = MFMA(fb[k % PD][j], fa[k % PD][i], first ? z4 : acc[j][i]);
      SB();
      if (k + PD < NF + NY) ld_b(k + PD, j);
      SB();
    }
    if (k + PD == NF - 1) yr = lds_ld<unsigned>(sm + SY_YRDY);
    if (k + PD == NF) {
      STAMP(5);
      lds_wait2<1, 0>(lds_addr(sm + SY_YRDY), yr, yr);
      STAMP(6);
    }
    if (k + PD < NF + NY) ld_a(k + PD);
    SB();
    if (k == NF - 1) {
      dump();
      bar();
      STAMP(7);
    }
  }
  STAMP(8);
  bar();
  dump();
  bar();
  STAMP(9);
}

template <bool BU>
DN void p1_loader(const Args& a, char* sm, int ti, int tj, int cg, int lw, int l) {
  const int l3 = l >> 3, l7 = l & 7, jb = tj - 9;
  const rsrc_t rx = mk_rsrc(a.x + (size_t)ti * 128 * D_, 128 * D_ * 2);
  const int vx = (BU ? (16 * (l3 >> 2) + (l3 & 3) + 4 * lw) : (16 * lw + l3)) * (D_ * 2) + ((l7 ^ l3) << 4);
  rsrc_t rw;
  int vw0, vw1 = 0, qmax = 5;
  if (BU) {
    rw = mk_rsrc(lw ? a.bim : a.bre, P_ * D_ * 2);
    vw0 = (96 * jb + l3) * (D_ * 2) + ((l7 ^ l3) << 4);
    qmax = (jb == 6) ? 3 : 5;
  } else {
    rw = mk_rsrc(a.w1, D_ * F_ * 2);
    const int f0 = (l3 >> 1) & 1;
    const int base = (32 * lw + l3) * (F_ * 2) + tj * 384 + (l7 & 1) * 16;
    vw0 = base + (((l7 >> 1) ^ f0) << 5);
    vw1 = base + (((l7 >> 1) ^ (f0 | 2)) << 5);
  }
  auto stage = [&](int s, int slot_i) NOPK {
    char* slot = sm + slot_i * SLOT;
#pragma unroll
    for (int v = 0; v < 2; ++v) {
#pragma unroll
      for (int q = 0; q < 4; ++q) {
        const int rowoff = BU ? (64 * (q >> 1) + 8 * (q & 1) + 32 * v) : (32 * q + 8 * v);
        glds16<0>(rx, slot + (4 * q + 2 * lw + v) * 1024, vx, rowoff * (D_ * 2) + s * 128);
      }
#pragma unroll
      for (int q = 0; q < 6; ++q) {
        if (BU) {
          const int qe = q < qmax ? q : qmax;
          glds16<0>(rw, slot + 16384 + (4 * q + 2 * lw + v) * 1024, vw0, (qe * 16 + 8 * v) * (D_ * 2) + s * 128);
        } else {
          glds16<0>(rw, slot + 16384 + (6 * (2 * lw + v) + q) * 1024, q < 3 ? vw0 : vw1,
                    (s * 64 + 16 * v + 8 * (q / 3)) * (F_ * 2) + (q % 3) * 128);
        }
      }
    }
  };
  const rsrc_t rb = mk_rsrc(a.ws + WS_W2F + cg * KSF * 5120, KSF * 5120);
  auto prefetch = [&](int slot_i) NOPK {
#pragma unroll
    for (int e = 0; e < 4; ++e) {
      const int gi = slot_i * 8 + lw * 4 + e, gw = gi / 6, gq = gi - 6 * gw;
      const int so = (kf_start(gw) + 2 * gq + 1) * 5120;
#pragma unroll
      for (int j = 0; j < 5; ++j) glds16<SC1>(rb, sm + gi * 5120 + j * 1024, l * 16, so + j * 1024);
    }
  };
  stage(0, 0);
  bar();
  stage(1, 1);
  WAIT_VM(20);
  lds_st(sm + SY_FULL + 4 * lw, 1u);
  stage(2, 2);
  int slot_i = 0;
#pragma unroll 1
  for (int t = 1; t < 8; ++t) {
    WAIT_VM(20);
    lds_st(sm + SY_FULL + 4 * lw, t + 1);
    lds_wait(sm + SY_FREE, t, lds_ld<u32x4>(sm + SY_FREE));
    stage(t + 2, slot_i);
    slot_i = slot_i == 2 ? 0 : slot_i + 1;
  }
  WAIT_VM(20);
  lds_st(sm + SY_FULL + 4 * lw, 9u);
  WAIT_VM(0);
  lds_st(sm + SY_FULL + 4 * lw, 10u);
  lds_wait(sm + SY_PTOK, 1u, lds_ld<unsigned>(sm + SY_PTOK));
  lds_wait(sm + SY_FREE, 8u, lds_ld<u32x4>(sm + SY_FREE));
  prefetch(1);
  lds_wait(sm + SY_FREE, 9u, lds_ld<u32x4>(sm + SY_FREE));
  prefetch(2);
  lds_wait(sm + SY_FREE, 10u, lds_ld<u32x4>(sm + SY_FREE));
  prefetch(0);
}

template <bool BU>
DN void p1_side(const Args& a, char* sm, int ti, int tj, int cg, int sw, int l) {
  const int jb = tj - 9, r = l & 15, g = l >> 4, l3 = l >> 3, l7 = l & 7;
  const int bl = ti * 16 + tj, pcg = bl & 7, pq = bl >> 3;
  const rsrc_t rws = mk_rsrc(a.ws, WS_END);
  if (sw == 0 && l < 32) lds_st(sm + LD_SYNC + 4 * l, l == 19 ? 1u : 0u);
  bar();
  __builtin_amdgcn_s_sleep(6);
  u32x4 va[20];
  {
    const char *p0, *p1;
    int step;
    if (sw == 0) {
      p0 = (const char*)(a.x + (size_t)(ti * 128 + 8 * tj + l3) * D_) + l7 * 16;
      p1 = p0;
      step = 128;
    } else if (BU) {
      const char* pp[2];
#pragma unroll
      for (int h = 0; h < 2; ++h) {
        int n = 12 * ti + 8 * h + l3;
        n = (8 * h + l3 < 12) ? n : 12 * ti;
        int ch = 96 * jb + 16 * (n >> 5) + (n & 15);
        ch = ch < P_ ? ch : P_ - 1;
        pp[h] = (const char*)(((n >> 4) & 1 ? a.bim : a.bre) + (size_t)ch * D_) + l7 * 16;
      }
      p0 = pp[0]; p1 = pp[1];
      step = 128;
    } else {
      const int lc = l < 48 ? l : l - 48, c = lc % 24, rg = lc / 24;
      p0 = (const char*)(a.w1 + (size_t)(40 * ti + 20 * rg) * F_ + 192 * tj) + c * 16;
      step = F_ * 2;
      p1 = p0 + 10 * step;
    }
#pragma unroll
    for (int i = 0; i < 10; ++i) va[i] = *(const u32x4*)(p0 + i * step);
#pragma unroll
    for (int i = 0; i < 10; ++i) va[10 + i] = *(const u32x4*)(p1 + i * (sw ? step : 0));
  }
  const u64 ep = flag_ld(a.ws + WS_EP);
  u16 c_ldt = 0, c_lre = 0, c_lim = 0;
  if (BU) {
    int p = 96 * jb + 48 * sw + (l < 48 ? l : 0);
    p = p < P_ ? p : P_ - 1;
    c_ldt = a.ldt[p]; c_lre = a.lre[p]; c_lim = a.lim[p];
  }
  SB();
  u32x4 vc[10];
  const int cks0 = 3 * (pq - 18), cfr = pq < 31 ? 15 : 5;
  auto piece = [&](int e) NOPK {
    const int pc = sw ? 10 + (e % 5) : e;
    return pc < cfr ? pc : cfr - 1;
  };
  if (pq < 18) {
    const rsrc_t r2 = mk_rsrc(a.w2, F_ * D_ * 2);
#pragma unroll
    for (int e = 0; e < 10; ++e) {
      const int pc = piece(e);
      const int q = pc * 64 + l, row = q / 10, ch = q - row * 10;
      if (e < 5 || sw == 0)
        glds16<0>(r2, sm + LD_AUX + pc * 1024, (96 * pq + row) * (D_ * 2) + pcg * 160 + ch * 16, 0);
    }
  } else {
    const u16* cm = g < 2 ? a.cre : a.cim;
#pragma unroll
    for (int e = 0; e < 10; ++e) {
      const int pc = piece(e), ksl = pc / 5, nt = pc - 5 * ksl;
      glds16_g(cm + (size_t)(80 * pcg + 16 * nt + r) * P_ + 16 * (cks0 + ksl) + 8 * (g & 1), sm + LD_AUX + pc * 1024,
               e < 5 || sw == 0);
    }
  }
  SB();
  if (sw == 0) {
    float s = 0.f;
#pragma unroll
    for (int i = 0; i < 10; ++i) s += sum8(va[i]);
    const float mean = red8(s, l) * (1.f / D_);
    float q = 0.f;
#pragma unroll
    for (int i = 0; i < 10; ++i)
#pragma unroll
      for (int e = 0; e < 4; ++e) {
        const float d0 = lo_f(va[i][e]) - mean, d1 = hi_f(va[i][e]) - mean;
        q = __builtin_fmaf(d0, d0, __builtin_fmaf(d1, d1, q));
      }
    const float var = red8(q, l) * (1.f / D_);
    const float sd = rbf(__builtin_sqrtf(rbf(rbf(var) + 1e-5f)));
    const u32x2 o = {as_u(rbf(mean)), as_u(sd)};
    if (l7 == 0) __builtin_amdgcn_raw_buffer_store_b64(o, rws, WS_STAT + (ti * 128 + 8 * tj + l3) * 8, 0, SC1);
    if (l == 0) *(u64*)(sm + SY_TAG) = ep + 0x9E3779B97F4A7C15ull;
    lds_st(sm + SY_TAG + 8, 1u);
  } else if (BU) {
#pragma unroll
    for (int h = 0; h < 2; ++h) {
      float s = 0.f;
#pragma unroll
      for (int i = 0; i < 10; ++i) s += sum8(va[10 * h + i]);
      s = red8(s, l);
      if (l7 == 0 && 8 * h + l3 < 12)
        __builtin_amdgcn_raw_buffer_store_b32(as_u(s), rws, WS_CSS + (jb * 192 + 12 * ti + 8 * h + l3) * 4, 0, SC1);
    }
  } else {
    float s[8];
#pragma unroll
    for (int e = 0; e < 8; ++e) s[e] = 0.f;
#pragma unroll
    for (int i = 0; i < 20; ++i)
#pragma unroll
      for (int e = 0; e < 4; ++e) { s[2 * e] += lo_f(va[i][e]); s[2 * e + 1] += hi_f(va[i][e]); }
#pragma unroll
    for (int e = 0; e < 8; ++e) s[e] += lane_get(s[e], l + 24);
    if (l < 24) {
#pragma unroll
      for (int h = 0; h < 2; ++h) {
        const u32x4 o = {as_u(s[4 * h]), as_u(s[4 * h + 1]), as_u(s[4 * h + 2]), as_u(s[4 * h + 3])};
        __builtin_amdgcn_raw_buffer_store_b128(o, rws, WS_CSF + ((tj * 16 + ti) * 192 + l * 8 + 4 * h) * 4, 0, SC1);
      }
    }
  }
  WAIT_VM(0);
  lds_wait(sm + SY_TAG + 8, 1u, lds_ld<unsigned>(sm + SY_TAG + 8));
  const u64 tag = *(const u64*)(sm + SY_TAG);
  if (l == 0) flag_st(a.ws + (sw == 0 ? WS_FST + (ti * 16 + tj) * 8 : WS_FCS + (tj * 16 + ti) * 8), tag);
  STAMP(12);
  if (pq < 18) {
#pragma unroll
    for (int e = 0; e < 10; ++e) {
      const int pc = piece(e), ksl = pc / 5, nt = pc - 5 * ksl;
      const char* b = sm + LD_AUX + (32 * ksl + 4 * g + (r >> 2)) * 160 + nt * 32 + (r & 3) * 8;
      vc[e] = __builtin_bit_cast(u32x4, tr_frag(b, b + 16 * 160));
    }
  } else {
#pragma unroll
    for (int e = 0; e < 10; ++e) vc[e] = *(const u32x4*)(sm + LD_AUX + piece(e) * 1024 + l * 16);
  }
  {
    const unsigned dst = pq < 18 ? WS_W2F + (pcg * KSF + 3 * pq) * 5120 : WS_CF + (pcg * KSY + cks0) * 5120;
#pragma unroll
    for (int e = 0; e < 10; ++e)
      if (e < 5 || sw == 0)
        __builtin_amdgcn_raw_buffer_store_b128(vc[e], rws, dst + piece(e) * 1024 + l * 16, 0, SC1);
  }
  if (BU) {
    cpx lam, coef;
    chan_consts(c_ldt, c_lre, c_lim, lam, coef);
    if (l < 48) *(f32x2*)(sm + LD_LAM + (48 * sw + l) * 8) = (f32x2){lam.re, lam.im};
  }
  WAIT_VM(0);
  if (l == 0) flag_st(a.ws + WS_FPT + ((pcg * 32 + pq) * 2 + sw) * 8, tag);
  STAMP(13);
  const char* fp = a.ws + (sw == 0 ? WS_FST + ti * 128 : WS_FCS + tj * 128) + r * 8;
  flag_wait(fp, true, tag, flag_ld(fp));
  u32x4 gv[12];
  if (sw == 0) {
    gv[0] = __builtin_amdgcn_raw_buffer_load_b128(rws, WS_STAT + ti * 1024 + l * 16, 0, SC1);
#pragma unroll
    for (int h = 0; h < 2; ++h) {
      const float mb = as_f(gv[0][2 * h]), sd = as_f(gv[0][2 * h + 1]);
      const float ri = 1.f / sd;
      ((float*)(sm + LD_RM))[2 * l + h] = BU ? mb * ri : mb;
      ((float*)(sm + LD_RI))[2 * l + h] = ri;
    }
  } else if (BU) {
    gv[0] = __builtin_amdgcn_raw_buffer_load_b128(rws, WS_CSS + jb * 768 + (l < 48 ? l : 0) * 16, 0, SC1);
    if (l < 48) *(u32x4*)(sm + LD_CS + l * 16) = gv[0];
  } else {
#pragma unroll
    for (int i = 0; i < 12; ++i)
      gv[i] = __builtin_amdgcn_raw_buffer_load_b128(
          rws, WS_CSF + (tj * 16 + 4 * (i / 3) + g) * 768 + (r + 16 * (i % 3)) * 16, 0, SC1);
#pragma unroll
    for (int c = 0; c < 3; ++c) {
      f32x4 s;
#pragma unroll
      for (int e = 0; e < 4; ++e) {
        float t = (as_f(gv[c][e]) + as_f(gv[3 + c][e])) + (as_f(gv[6 + c][e]) + as_f(gv[9 + c][e]));
        t += lane_get(t, l ^ 16);
        t += lane_get(t, l ^ 32);
        s[e] = t;
      }
      if (g == 0) *(f32x4*)(sm + LD_CS + (r + 16 * c) * 16) = s;
    }
  }
  asm volatile("" ::: "memory");
  lds_st(sm + SY_GATH + 4 * sw, 1u);
  STAMP(14);
  if (sw == 0) {
    const char* fpt = a.ws + WS_FPT + cg * 512 + l * 8;
    flag_wait(fpt, true, tag, flag_ld(fpt));
    lds_st(sm + SY_PTOK, 1u);
  }
  STAMP(15);
}

struct JobC { cpx l1, cf, l256, fac, lg, cl; };
struct JobD { u32x2 e[8], pre; u32x4 h[4]; };
DN void job_consts(const Args& a, int rt, int ct, int l, JobC& k) {
  const int c = l & 15, g = l >> 4, p = 16 * ct + c;
  cpx cf;
  chan_consts(a.ldt[p], a.lre[p], a.lim[p], k.l1, cf);
  k.cf = {2.f * cf.re, 2.f * cf.im};
  const cpx l2 = c_mul(k.l1, k.l1), l4 = c_mul(l2, l2), l8 = c_mul(l4, l4), l16 = c_mul(l8, l8), l32 = c_mul(l16, l16),
            l64 = c_mul(l32, l32), l128 = c_mul(l64, l64);
  k.l256 = c_mul(l128, l128);
  const int ex = (rt - 1 - g) & 3;
  const cpx one = {1.f, 0.f};
  k.fac = c_mul(c_sel(ex & 1, l64, one), c_sel(ex & 2, l128, one));
  k.lg = c_mul(c_sel(g & 1, l16, one), c_sel(g & 2, l32, one));
  k.cl = c_mul(k.cf, k.l1);
  asm volatile("" : "+v"(k.l1.re), "+v"(k.l1.im), "+v"(k.cf.re), "+v"(k.cf.im), "+v"(k.l256.re), "+v"(k.l256.im),
                    "+v"(k.fac.re), "+v"(k.fac.im), "+v"(k.lg.re), "+v"(k.lg.im), "+v"(k.cl.re), "+v"(k.cl.im));
}
DN void job_load(rsrc_t rws, int rt, int ct, int l, JobD& d) {
  const int c = l & 15, g = l >> 4;
#pragma unroll
  for (int i = 0; i < 8; ++i)
    d.e[i] = __builtin_amdgcn_raw_buffer_load_b64(rws, WS_E64 + ((ct * 32 + 4 * i + g) * 16 + c) * 8, 0, SC1);
  d.pre = __builtin_amdgcn_raw_buffer_load_b64(rws, WS_PPRE + ((rt * KSY + ct) * 64 + l) * 8, 0, SC1);
#pragma unroll
  for (int q = 0; q < 4; ++q)
    d.h[q] = __builtin_amdgcn_raw_buffer_load_b128(rws, WS_HLOC + ((rt * KSY + ct) * 4 + q) * 1024 + l * 16, 0, SC1);
}
DN void job_run(rsrc_t rws, char* scr, int rt, int ct, int l, const JobC& k, const JobD& d) {
  const int c = l & 15, g = l >> 4;
  cpx H = {0.f, 0.f};
#pragma unroll
  for (int i = 0; i < 8; ++i) H = c_sel(4 * i + g < rt, c_fma(k.l256, H, cpx{as_f(d.e[i][0]), as_f(d.e[i][1])}), H);
  cpx cr = c_mul(k.fac, H);
  cr.re += lane_get(cr.re, l ^ 16); cr.im += lane_get(cr.im, l ^ 16);
  cr.re += lane_get(cr.re, l ^ 32); cr.im += lane_get(cr.im, l ^ 32);
  const cpx carry = c_fma(k.lg, cr, cpx{as_f(d.pre[0]), as_f(d.pre[1])});
  cpx G = c_mul(k.cl, carry);
  u32x4 ore[2], oim[2];
#pragma unroll
  for (int q = 0; q < 2; ++q)
#pragma unroll
    for (int i = 0; i < 4; ++i) {
      const cpx a0 = c_fma(k.cf, cpx{lo_f(d.h[q][i]), lo_f(d.h[2 + q][i])}, G);
      G = c_mul(k.l1, G);
      const cpx a1 = c_fma(k.cf, cpx{hi_f(d.h[q][i]), hi_f(d.h[2 + q][i])}, G);
      G = c_mul(k.l1, G);
      ore[q][i] = pk_bf16(a0.re, a1.re);
      oim[q][i] = pk_bf16(-a0.im, -a1.im);
    }
#pragma unroll
  for (int q = 0; q < 2; ++q) {
    *(u32x4*)(scr + c * 144 + g * 32 + q * 16) = ore[q];
    *(u32x4*)(scr + (16 + c) * 144 + g * 32 + q * 16) = oim[q];
  }
  const char* b = scr + (8 * g + (c >> 2)) * 144 + (c & 3) * 8;
  u32x4 v[4];
#pragma unroll
  for (int mt = 0; mt < 4; ++mt) v[mt] = __builtin_bit_cast(u32x4, tr_frag(b + mt * 32, b + mt * 32 + 4 * 144));
#pragma unroll
  for (int mt = 0; mt < 4; ++mt)
    __builtin_amdgcn_raw_buffer_store_b128(v[mt], rws, WS_HF + ((rt * KSY + ct) * 4 + mt) * 1024 + l * 16, 0, SC1);
}

DN void p2_helper(const Args& a, char* sm, int rt, int cg, int hw, int l) {
  const u64 tag = *(const u64*)(sm + SY_TAG);
  const rsrc_t rws = mk_rsrc(a.ws, WS_END);
  const int ht = hw * 64 + l;
  const int ct0 = 5 * cg + hw, ct1 = 5 * cg + 4;
  JobC k0, k1;
  job_consts(a, rt, ct0, l, k0);
  if (hw == 3) job_consts(a, rt, ct1, l, k1);
  {
    const char* fp = a.ws + WS_FST + ((rt >> 1) * 16 + (l & 15)) * 8;
    flag_wait(fp, true, tag, flag_ld(fp));
  }
  u32x2 st[5], xv[5], dv[5];
#pragma unroll
  for (int e = 0; e < 5; ++e) {
    const int idx = ht + 256 * e, row = idx / 20, c4 = idx - row * 20;
    const int t = rt * 64 + row, d = cg * 80 + c4 * 4;
    st[e] = __builtin_amdgcn_raw_buffer_load_b64(rws, WS_STAT + t * 8, 0, SC1);
    xv[e] = *(const u32x2*)(a.x + (size_t)t * D_ + d);
    dv[e] = *(const u32x2*)(a.dsk + d);
  }
  SB();
  WAIT_VM(0);
  if (hw < 2) lds_st(sm + SY_P2GO + 4 * hw, 1u);
  float xs[5][4], xd[5][4], gt[5][4], ri[5];
#pragma unroll
  for (int e = 0; e < 5; ++e) {
    xs[e][0] = lo_f(xv[e][0]); xs[e][1] = hi_f(xv[e][0]); xs[e][2] = lo_f(xv[e][1]); xs[e][3] = hi_f(xv[e][1]);
    const float ds[4] = {lo_f(dv[e][0]), hi_f(dv[e][0]), lo_f(dv[e][1]), hi_f(dv[e][1])};
    const float mb = as_f(st[e][0]), sd = as_f(st[e][1]);
    ri[e] = 1.f / sd;
#pragma unroll
    for (int i = 0; i < 4; ++i) xd[e][i] = rbf(rbf(rbf(xs[e][i] - mb) / sd) * ds[i]);
    asm volatile("" : "+v"(xd[e][0]), "+v"(xd[e][1]), "+v"(xd[e][2]), "+v"(xd[e][3]), "+v"(ri[e]));
  }
  STAMP(3);
  if (hw == 2) {
    const char* fp = a.ws + WS_FAF + (rt * 32 + (l & 31)) * 8;
    flag_wait(fp, (l & 31) < 18, tag, flag_ld(fp));
    lds_st(sm + SY_P2GO + 8, 1u);
  }
  STAMP(4);
  {
    const int ctf = (l < 32 || hw != 3) ? ct0 : ct1;
    const char* fp = a.ws + WS_FAS + ((ctf / 3) * 32 + (l & 31)) * 8;
    flag_wait(fp, (l & 31) <= rt, tag, flag_ld(fp));
  }
  STAMP(16);
  JobD d0, d1;
  job_load(rws, rt, ct0, l, d0);
  if (hw == 3) job_load(rws, rt, ct1, l, d1);
  SB();
  job_run(rws, sm + LD_AUX + hw * 4608, rt, ct0, l, k0, d0);
  if (hw == 3) job_run(rws, sm + LD_AUX + hw * 4608, rt, ct1, l, k1, d1);
  STAMP(17);
  WAIT_VM(0);
  if (l == 0) flag_st(a.ws + WS_FH + (rt * 32 + cg * 4 + hw) * 8, tag);
  STAMP(5);
  if (hw == 0) {
    const char* fp = a.ws + WS_FH + (rt * 32 + (l & 31)) * 8;
    flag_wait(fp, true, tag, flag_ld(fp));
    lds_st(sm + SY_YRDY, 1u);
  }
  if (blockIdx.x == 0 && hw == 1) {
#pragma unroll
    for (int i = 0; i < 4; ++i) {
      const char* fp = a.ws + WS_FST + (i * 64 + l) * 8;
      flag_wait(fp, true, tag, flag_ld(fp));
    }
    if (l == 0) flag_st(a.ws + WS_EP, tag);
  }
  STAMP(6);
  bar();
  STAMP(7);
#pragma unroll
  for (int e = 0; e < 5; ++e) {
    const int idx = ht + 256 * e, row = idx / 20, c4 = idx - row * 20;
    const char* p = sm + row * RED_ROW + c4 * 16;
    const f32x4 p0 = *(const f32x4*)p, p1 = *(const f32x4*)(p + WREG), p2 = *(const f32x4*)(p + 2 * WREG),
                p3 = *(const f32x4*)(p + 3 * WREG);
#pragma unroll
    for (int i = 0; i < 4; ++i) {
      const float fv = rbf(((p0[i] + p1[i]) + (p2[i] + p3[i])) * ri[e]);
      gt[e][i] = rbf(fv / (1.f + __builtin_amdgcn_exp2f(-1.44269504088896f * fv)));
    }
    asm volatile("" : "+v"(gt[e][0]), "+v"(gt[e][1]), "+v"(gt[e][2]), "+v"(gt[e][3]));
  }
  STAMP(8);
  bar();
  bar();
  STAMP(9);
#pragma unroll
  for (int e = 0; e < 5; ++e) {
    const int idx = ht + 256 * e, row = idx / 20, c4 = idx - row * 20;
    const char* p = sm + row * RED_ROW + c4 * 16;
    const f32x4 p0 = *(const f32x4*)p, p1 = *(const f32x4*)(p + WREG), p2 = *(const f32x4*)(p + 2 * WREG),
                p3 = *(const f32x4*)(p + 3 * WREG);
    f32x4 o4;
#pragma unroll
    for (int i = 0; i < 4; ++i) o4[i] = xs[e][i] + (((p0[i] + p1[i]) + (p2[i] + p3[i])) + xd[e][i]) * gt[e][i];
    *(f32x4*)(a.out + (size_t)(rt * 64 + row) * D_ + cg * 80 + c4 * 4) = o4;
  }
}

__global__ __launch_bounds__(512, 1) NOPK void fused_kernel(Args a) {
  extern __shared__ __attribute__((aligned(16))) char sm[];
  const int tid = threadIdx.x, l = tid & 63;
  const int w = __builtin_amdgcn_readfirstlane(tid >> 6);
  const int b = blockIdx.x, x = b & 7, o = b >> 3;
  const int ti = (x & 1) * 8 + (o & 7);
  const int tj = (x >> 1) * 4 + (o >> 3);
  const int rt = (x >> 1) * 8 + (o >> 2);
  const int cg = (x & 1) * 4 + (o & 3);
  STAMP(0);
  if (w < 4) {
    if (tj < 9) p1_gemm<false>(a, sm, ti, tj, w, l);
    else p1_gemm<true>(a, sm, ti, tj, w, l);
    p2_gemm(a, sm, rt, cg, w, l);
  } else {
    __builtin_amdgcn_s_setprio(2);
    if (w < 6) {
      if (tj < 9) p1_loader<false>(a, sm, ti, tj, cg, w - 4, l);
      else p1_loader<true>(a, sm, ti, tj, cg, w - 4, l);
    } else {
      if (tj < 9) p1_side<false>(a, sm, ti, tj, cg, w - 6, l);
      else p1_side<true>(a, sm, ti, tj, cg, w - 6, l);
    }
    STAMP(2);
    p2_helper(a, sm, rt, cg, w - 4, l);
  }
  STAMP(10);
  STAMP(11);
}

extern "C" void kernel_launch(void* const* d_in, const int* in_sizes, int n_in, void* d_out, int out_size,
                              void* d_ws, size_t ws_size, hipStream_t stream) {
  (void)in_sizes; (void)n_in; (void)out_size; (void)ws_size;
  Args a;
  a.x = (const u16*)d_in[0]; a.lre = (const u16*)d_in[1]; a.lim = (const u16*)d_in[2];
  a.bre = (const u16*)d_in[3]; a.bim = (const u16*)d_in[4]; a.cre = (const u16*)d_in[5];
  a.cim = (const u16*)d_in[6]; a.ldt = (const u16*)d_in[7]; a.dsk = (const u16*)d_in[8];
  a.w1 = (const u16*)d_in[10]; a.w2 = (const u16*)d_in[11];
  a.ws = (char*)d_ws; a.out = (float*)d_out;
  (void)hipFuncSetAttribute((const void*)fused_kernel, hipFuncAttributeMaxDynamicSharedMemorySize, LDS_BYTES);
  fused_kernel<<<256, 512, LDS_BYTES, stream>>>(a);
}
```

```cpp
#define NOTOUCH
#define GATHER_AT 7u
#define POLL_SLEEP 10
#define PROBE2 2800
#include <hip/hip_runtime.h>
#include <stdint.h>

typedef unsigned short u16;
typedef unsigned long long u64;
typedef __attribute__((ext_vector_type(8))) short bf16x8;
typedef __attribute__((ext_vector_type(4))) short s16x4;
typedef __attribute__((ext_vector_type(4))) float f32x4;
typedef __attribute__((ext_vector_type(2))) float f32x2;
typedef __attribute__((ext_vector_type(4))) unsigned u32x4;
typedef __attribute__((ext_vector_type(2))) unsigned u32x2;
typedef __attribute__((ext_vector_type(2))) __bf16 bf16x2;
typedef __amdgpu_buffer_rsrc_t rsrc_t;
#define LDS_AS __attribute__((address_space(3)))
#if defined(__HIP_DEVICE_COMPILE__)
#define NOPK __attribute__((target("no-packed-fp32-ops")))
#else
#define NOPK
#endif
#define DN __device__ __forceinline__ NOPK
#define MFMA(a, b, c) __builtin_amdgcn_mfma_f32_16x16x32_bf16((a), (b), (c), 0, 0, 0)
#define SB() __builtin_amdgcn_sched_barrier(0)
#define WAIT_VM(n) asm volatile("s_waitcnt vmcnt(" #n ")" ::: "memory")
#define SC1 16
template <int V> struct IC { static constexpr int value = V; };

constexpr int L_ = 2048, D_ = 640, P_ = 640, F_ = 1728;
constexpr int KSF = F_ / 32;
constexpr int KSY = P_ / 16;

constexpr unsigned WS_H1F = 0;
constexpr unsigned WS_HLOC = WS_H1F + 32u * KSF * 4096;
constexpr unsigned WS_HF = WS_HLOC + 32u * KSY * 4096;
constexpr unsigned WS_W2F = WS_HF + 32u * KSY * 4096;
constexpr unsigned WS_CF = WS_W2F + 8u * KSF * 5120;
constexpr unsigned WS_PPRE = WS_CF + 8u * KSY * 5120;
constexpr unsigned WS_E64 = WS_PPRE + 32u * KSY * 512;
constexpr unsigned WS_STAT = WS_E64 + KSY * 32u * 128;
constexpr unsigned WS_CSF = WS_STAT + L_ * 8u;
constexpr unsigned WS_CSS = WS_CSF + 9u * 16 * 768;
constexpr unsigned WS_FST = WS_CSS + 7u * 768 + 768;
constexpr unsigned WS_FCS = WS_FST + 2048;
constexpr unsigned WS_FPT = WS_FCS + 2048;
constexpr unsigned WS_FAF = WS_FPT + 4096;
constexpr unsigned WS_FAS = WS_FAF + 8192;
constexpr unsigned WS_FH = WS_FAS + 4096;
constexpr unsigned WS_EP = WS_FH + 8192;
constexpr unsigned WS_END = WS_EP + 128;

constexpr int SLOT = 40960;
constexpr int LD_AUX = 3 * SLOT;
constexpr int LD_RM = LD_AUX + 18432;
constexpr int LD_RI = LD_RM + 512;
constexpr int LD_CS = LD_RI + 512;
constexpr int LD_LAM = LD_CS + 768;
constexpr int LD_ST2 = LD_LAM + 768;
constexpr int LD_SYNC = LD_ST2 + 512;
constexpr int LDS_BYTES = LD_SYNC + 128;
constexpr int SY_TAG = LD_SYNC, SY_FULL = LD_SYNC + 16, SY_FREE = LD_SYNC + 32, SY_GATH = LD_SYNC + 48,
              SY_PTOK = LD_SYNC + 56, SY_P2GO = LD_SYNC + 64, SY_YRDY = LD_SYNC + 80;
constexpr int WREG = 30720;
constexpr int RED_ROW = 336;
constexpr int PD = 3, NF = 14, NY = 10;
constexpr unsigned SPIN_MAX = 1u << 18;

#define STAMP(i)

struct Args {
  const u16 *x, *lre, *lim, *bre, *bim, *cre, *cim, *ldt, *dsk, *w1, *w2;
  char* ws;
  float* out;
};

struct cpx { float re, im; };
DN cpx c_mul(cpx a, cpx b) { return {a.re * b.re - a.im * b.im, a.re * b.im + a.im * b.re}; }
DN cpx c_fma(cpx a, cpx b, cpx c) {
  return {__builtin_fmaf(a.re, b.re, __builtin_fmaf(-a.im, b.im, c.re)),
          __builtin_fmaf(a.re, b.im, __builtin_fmaf(a.im, b.re, c.im))};
}
DN cpx c_sel(bool p, cpx a, cpx b) { return {p ? a.re : b.re, p ? a.im : b.im}; }
DN float lane_get(float v, int src) {
  return __builtin_bit_cast(float, __builtin_amdgcn_ds_bpermute(src << 2, __builtin_bit_cast(int, v)));
}
DN unsigned pk_bf16(float lo, float hi) {
  f32x2 f = {lo, hi};
  return __builtin_bit_cast(unsigned, __builtin_convertvector(f, bf16x2));
}
DN float as_f(unsigned u) { return __builtin_bit_cast(float, u); }
DN unsigned as_u(float f) { return __builtin_bit_cast(unsigned, f); }
DN float lo_f(unsigned u) { return __builtin_bit_cast(float, u << 16); }
DN float hi_f(unsigned u) { return __builtin_bit_cast(float, u & 0xffff0000u); }
DN float bf2f(u16 v) { return lo_f(v); }
DN float rbf(float f) { return lo_f(pk_bf16(f, 0.f)); }
DN rsrc_t mk_rsrc(const void* p, unsigned bytes) {
  return __builtin_amdgcn_make_buffer_rsrc((void*)p, 0, bytes, 0x00020000);
}
template <int AUX>
DN void glds16(rsrc_t r, char* lds, int voff, int soff) {
  __builtin_amdgcn_raw_ptr_buffer_load_lds(r, (LDS_AS void*)lds, 16, voff, soff, 0, AUX);
}
#define BARS(i) bar()
DN void glds16_g(const void* src, char* lds, bool on) {
  if (on) __builtin_amdgcn_global_load_lds((const __attribute__((address_space(1))) void*)src, (LDS_AS void*)lds, 16, 0, 0);
}
DN void bar() {
  asm volatile("s_waitcnt lgkmcnt(0)" ::: "memory");
  __builtin_amdgcn_s_barrier();
  asm volatile("" ::: "memory");
  SB();
}
template <class T> DN T lds_ld(const char* p) { return *(volatile LDS_AS T*)(LDS_AS char*)p; }
DN void lds_st(char* p, unsigned v) {
  asm volatile("" ::: "memory");
  *(volatile LDS_AS unsigned*)(LDS_AS char*)p = v;
  asm volatile("" ::: "memory");
}
DN unsigned vmin(u32x2 v) { return v[0] < v[1] ? v[0] : v[1]; }
DN unsigned vmin(u32x4 v) {
  const unsigned a = v[0] < v[1] ? v[0] : v[1], b = v[2] < v[3] ? v[2] : v[3];
  return a < b ? a : b;
}
DN unsigned vmin(unsigned v) { return v; }
template <class T> DN void lds_wait(const char* p, unsigned n, T first) {
  unsigned spins = 0;
  while (__builtin_amdgcn_readfirstlane(vmin(first)) < n && spins < 64u * SPIN_MAX) {
    __builtin_amdgcn_s_sleep(1);
    first = lds_ld<T>(p);
    ++spins;
  }
  asm volatile("" ::: "memory");
}
template <int N, int OFF2>
DN void lds_wait2(unsigned addr, unsigned f0, unsigned f1) {
  unsigned s0, s1, cnt;
  asm volatile(
      "s_mov_b32 %4, 0\n"
      "1:\n\t"
      "v_readfirstlane_b32 %2, %0\n\t"
      "v_readfirstlane_b32 %3, %1\n\t"
      "s_add_u32 %4, %4, 1\n\t"
      "s_min_u32 %2, %2, %3\n\t"
      "s_cmp_ge_u32 %2, %6\n\t"
      "s_cbranch_scc1 2f\n\t"
      "s_bitcmp1_b32 %4, 24\n\t"
      "s_cbranch_scc1 2f\n\t"
      "s_sleep 1\n\t"
      "ds_read_b32 %0, %5\n\t"
      "ds_read_b32 %1, %5 offset:%7\n\t"
      "s_waitcnt lgkmcnt(0)\n\t"
      "s_branch 1b\n"
      "2:"
      : "+v"(f0), "+v"(f1), "=&s"(s0), "=&s"(s1), "=&s"(cnt)
      : "v"(addr), "i"(N), "i"(OFF2)
      : "scc", "memory");
}
DN unsigned lds_addr(const char* p) { return (unsigned)(uintptr_t)(LDS_AS const char*)p; }
DN bf16x8 tr_frag(const char* p0, const char* p1) {
  s16x4 lo = __builtin_amdgcn_ds_read_tr16_b64_v4i16((LDS_AS s16x4*)p0);
  s16x4 hi = __builtin_amdgcn_ds_read_tr16_b64_v4i16((LDS_AS s16x4*)p1);
  return __builtin_shufflevector(lo, hi, 0, 1, 2, 3, 4, 5, 6, 7);
}
DN u64 flag_ld(const char* p) { return __hip_atomic_load((const u64*)p, __ATOMIC_RELAXED, __HIP_MEMORY_SCOPE_AGENT); }
DN void flag_st(char* p, u64 v) { __hip_atomic_store((u64*)p, v, __ATOMIC_RELAXED, __HIP_MEMORY_SCOPE_AGENT); }
DN void flag_wait(const char* p, bool need, u64 tag) {
  unsigned spins = 0;
  for (;;) {
    if (need) need = flag_ld(p) != tag;
    if (__builtin_amdgcn_ballot_w64(need) == 0 || ++spins >= SPIN_MAX) break;
    __builtin_amdgcn_s_sleep(POLL_SLEEP);
  }
  __builtin_amdgcn_fence(__ATOMIC_ACQUIRE, "wavefront");
}
DN float exp_acc(float x) {
  const float l2e = 1.44269502162933349609375f, l2e_lo = 1.925963033500011e-08f;
  const float t = x * l2e;
  const float e = __builtin_fmaf(x, l2e_lo, __builtin_fmaf(x, l2e, -t));
  const float r = __builtin_amdgcn_exp2f(t);
  return __builtin_fmaf(r, e * 0.693147180559945f, r);
}
DN void sincos_acc(float x, float& s, float& c) {
  const float k = __builtin_rintf(x * 0.636619772367581f);
  float r = __builtin_fmaf(k, -1.57079601287841796875f, x);
  r = __builtin_fmaf(k, -3.1391647326017846353352069854736328125e-7f, r);
  r = __builtin_fmaf(k, -5.390302529957764765544681040410068817436695098876953125e-15f, r);
  const float z = r * r;
  float ps = __builtin_fmaf(z, 2.7183114939898219064e-6f, -1.98393348360966317347e-4f);
  ps = __builtin_fmaf(z, ps, 8.3333293858894631756e-3f);
  ps = __builtin_fmaf(z, ps, -1.66666666416265235595e-1f);
  const float sn = __builtin_fmaf(r * z, ps, r);
  float pc = __builtin_fmaf(z, 2.43904487962774090654e-5f, -1.38867637746099294692e-3f);
  pc = __builtin_fmaf(z, pc, 4.16666233237390631894e-2f);
  pc = __builtin_fmaf(z, pc, -4.99999997251031003120e-1f);
  const float cs = __builtin_fmaf(z, pc, 1.f);
  const int q = (int)k;
  const float s0 = (q & 1) ? cs : sn, c0 = (q & 1) ? sn : cs;
  s = (q & 2) ? -s0 : s0;
  c = ((q + 1) & 2) ? -c0 : c0;
}
DN void chan_consts(u16 ldt, u16 lre, u16 lim, cpx& lam, cpx& coef) {
  const float dt = rbf(exp_acc(bf2f(ldt)));
  const float lr = bf2f(lre), li = bf2f(lim);
  const float e = exp_acc(dt * lr);
  float sn, cs;
  sincos_acc(dt * li, sn, cs);
  lam = {e * cs, e * sn};
  const float nr = lam.re - 1.f, ni = lam.im, den = lr * lr + li * li;
  coef = {(nr * lr + ni * li) / den, (ni * lr - nr * li) / den};
}
DN float sum8(u32x4 v) {
  return ((lo_f(v[0]) + hi_f(v[0])) + (lo_f(v[1]) + hi_f(v[1]))) + ((lo_f(v[2]) + hi_f(v[2])) + (lo_f(v[3]) + hi_f(v[3])));
}
DN float red8(float v, int l) {
  v += lane_get(v, l ^ 1);
  v += lane_get(v, l ^ 2);
  v += lane_get(v, l ^ 4);
  return v;
}
DN int kf_start(int w) { return w < 2 ? NF * w : 2 * NF + (NF - 1) * (w - 2); }

template <bool BU>
DN void p1_gemm(const Args& a, char* sm, int ti, int tj, int w, int l) {
  const int wm = w >> 1, wn = w & 1, r = l & 15, g = l >> 4, jb = tj - 9;
  int xo[2], wo[2][2];
#pragma unroll
  for (int kk = 0; kk < 2; ++kk) {
    xo[kk] = (64 * wm + r) * 128 + (((4 * kk + g) ^ (r & 7)) << 4);
    if (BU) {
      wo[kk][0] = 16384 + (96 * wn + r) * 128 + (((4 * kk + g) ^ (r & 7)) << 4);
      wo[kk][1] = 0;
    } else {
      const int q = r >> 2, p = r & 3, f = (q >> 1) | ((g & 1) << 1);
      const int base = 16384 + (4 * kk + g) * 3072 + q * 128 + 8 * p;
      wo[kk][0] = base + (((2 * wn) ^ f) << 5);
      wo[kk][1] = base + (((2 * wn + 1) ^ f) << 5);
    }
  }
  bf16x8 xf[2][4], wf[2][6];
  auto rd = [&](int s, int h, int i) NOPK {
    const char* slot = sm + (s % 3) * SLOT;
    if (i >= 1 && i <= 4) {
      xf[h][i - 1] = *(const bf16x8*)(slot + xo[h] + (i - 1) * 2048);
    } else {
      const int nt = i == 0 ? 0 : i - 4;
      if (BU) wf[h][nt] = *(const bf16x8*)(slot + wo[h][0] + nt * 2048);
      else {
        const char* b = slot + wo[h][nt & 1] + (nt >> 1) * 1024;
        wf[h][nt] = tr_frag(b, b + 512);
      }
    }
  };
  f32x4 acc[6][4];
  const f32x4 z4 = {0.f, 0.f, 0.f, 0.f};
  u32x2 fl = {0u, 0u};
  auto half = [&](int h, bool first, bool pre, int ps, int ph) NOPK {
#pragma unroll
    for (int n = 0; n < 6; ++n)
#pragma unroll
      for (int m = 0; m < 4; ++m) {
        const int idx = 4 * n + m;
        if (pre && idx < 10) rd(ps, ph, idx);
        if (h == 0 && idx == 10) lds_st(sm + SY_FREE + 4 * w, ps + 1);
        if (h == 0 && idx == 16) fl = lds_ld<u32x2>(sm + (ps < 9 ? SY_FULL : SY_GATH));
        if (BU) acc[n][m] = MFMA(xf[h][m], wf[h][n], first ? z4 : acc[n][m]);
        else acc[n][m] = MFMA(wf[h][n], xf[h][m], first ? z4 : acc[n][m]);
        if (idx < 12 || m == 3 || idx == 16) SB();
      }
  };
  bar();
  lds_wait2<1, 0>(lds_addr(sm + SY_FULL), 0u, 0u);
  STAMP(1);
#pragma unroll
  for (int i = 0; i < 10; ++i) rd(0, 0, i);
  SB();
  auto stage = [&](auto sc) NOPK {
    constexpr int s = decltype(sc)::value;
    half(0, s == 0, true, s, 1);
    if (s < 9) lds_wait2<s + 2, 0>(lds_addr(sm + SY_FULL + 4 * ((s + 1) & 1)), fl[(s + 1) & 1], fl[(s + 1) & 1]);
    else lds_wait2<1, 4>(lds_addr(sm + SY_GATH), fl[0], fl[1]);
    SB();
    half(1, false, s < 9, s + 1, 0);
  };
  stage(IC<0>{}); stage(IC<1>{}); stage(IC<2>{}); stage(IC<3>{}); stage(IC<4>{});
  stage(IC<5>{}); stage(IC<6>{}); stage(IC<7>{}); stage(IC<8>{}); stage(IC<9>{});
  STAMP(2);

  const u64 tag = *(const u64*)(sm + SY_TAG);
  const float* RM = (const float*)(sm + LD_RM);
  const float* RI = (const float*)(sm + LD_RI);
  const float* CS = (const float*)(sm + LD_CS);
  const int rt = 2 * ti + wm;
  const rsrc_t rws = mk_rsrc(a.ws, WS_END);
  if (!BU) {
    float mu[4];
#pragma unroll
    for (int m = 0; m < 4; ++m) mu[m] = RM[64 * wm + 16 * m + r];
#pragma unroll
    for (int pc = 0; pc < 3; ++pc) {
      const f32x4 c0 = *(const f32x4*)(CS + 64 * pc + 32 * wn + 4 * g);
      const f32x4 c1 = *(const f32x4*)(CS + 64 * pc + 32 * wn + 16 + 4 * g);
      const int ks = 6 * tj + 2 * pc + wn;
#pragma unroll
      for (int m = 0; m < 4; ++m) {
        float v[8];
#pragma unroll
        for (int e = 0; e < 4; ++e) {
          v[e] = __builtin_fmaxf(__builtin_fmaf(-c0[e], mu[m], acc[2 * pc][m][e]), 0.f);
          v[4 + e] = __builtin_fmaxf(__builtin_fmaf(-c1[e], mu[m], acc[2 * pc + 1][m][e]), 0.f);
        }
        const u32x4 o = {pk_bf16(v[0], v[1]), pk_bf16(v[2], v[3]), pk_bf16(v[4], v[5]), pk_bf16(v[6], v[7])};
        __builtin_amdgcn_raw_buffer_store_b128(o, rws, WS_H1F + ((rt * KSF + ks) * 4 + m) * 1024 + l * 16, 0, SC1);
      }
    }
    lds_st(sm + SY_FREE + 4 * w, 11u);
    STAMP(22);
    WAIT_VM(0);
    if (l == 0) flag_st(a.ws + WS_FAF + (rt * 32 + 2 * tj + wn) * 8, tag);
  } else {
    f32x4 rm[4], ri[4];
#pragma unroll
    for (int m = 0; m < 4; ++m) {
      rm[m] = *(const f32x4*)(RM + 64 * wm + 16 * g + 4 * m);
      ri[m] = *(const f32x4*)(RI + 64 * wm + 16 * g + 4 * m);
    }
    cpx lam[3], h[3];
    float csr[3], csi[3];
#pragma unroll
    for (int c = 0; c < 3; ++c) {
      const int ctl = 3 * wn + c;
      const f32x2 lv = *(const f32x2*)(sm + LD_LAM + (16 * ctl + r) * 8);
      lam[c] = {lv[0], lv[1]};
      csr[c] = CS[32 * ctl + r];
      csi[c] = CS[32 * ctl + 16 + r];
      h[c] = {0.f, 0.f};
    }
#pragma unroll
    for (int m = 0; m < 4; ++m)
#pragma unroll
      for (int e = 0; e < 4; ++e)
#pragma unroll
        for (int c = 0; c < 3; ++c) {
          const float ur = __builtin_fmaf(acc[2 * c][m][e], ri[m][e], -rm[m][e] * csr[c]);
          const float ui = __builtin_fmaf(acc[2 * c + 1][m][e], ri[m][e], -rm[m][e] * csi[c]);
          h[c] = c_fma(lam[c], h[c], cpx{ur, ui});
          acc[2 * c][m][e] = h[c].re;
          acc[2 * c + 1][m][e] = h[c].im;
        }
    lds_st(sm + SY_FREE + 4 * w, 11u);
    const int nct = (jb == 6 && wn == 1) ? 1 : 3;
#pragma unroll
    for (int c = 0; c < 3; ++c) {
      const int ct = 6 * jb + 3 * wn + c;
      if (c < nct) {
#pragma unroll
        for (int q = 0; q < 4; ++q) {
          const f32x4 v0 = acc[2 * c + (q >> 1)][2 * (q & 1)], v1 = acc[2 * c + (q >> 1)][2 * (q & 1) + 1];
          const u32x4 o = {pk_bf16(v0[0], v0[1]), pk_bf16(v0[2], v0[3]), pk_bf16(v1[0], v1[1]), pk_bf16(v1[2], v1[3])};
          __builtin_amdgcn_raw_buffer_store_b128(o, rws, WS_HLOC + ((rt * KSY + ct) * 4 + q) * 1024 + l * 16, 0, SC1);
        }
      }
    }
#pragma unroll
    for (int c = 0; c < 3; ++c) {
      const int ct = 6 * jb + 3 * wn + c;
      const cpx l2 = c_mul(lam[c], lam[c]), l4 = c_mul(l2, l2), l8 = c_mul(l4, l4), l16 = c_mul(l8, l8), l32 = c_mul(l16, l16);
      cpx v = h[c];
      const cpx u1 = {lane_get(v.re, l - 16), lane_get(v.im, l - 16)};
      v = c_sel(g >= 1, c_fma(l16, u1, v), v);
      const cpx u2 = {lane_get(v.re, l - 32), lane_get(v.im, l - 32)};
      v = c_sel(g >= 2, c_fma(l32, u2, v), v);
      cpx ex = {lane_get(v.re, l - 16), lane_get(v.im, l - 16)};
      ex = c_sel(g == 0, cpx{0.f, 0.f}, ex);
      if (c < nct) {
        const u32x2 pv = {as_u(ex.re), as_u(ex.im)};
        __builtin_amdgcn_raw_buffer_store_b64(pv, rws, WS_PPRE + ((rt * KSY + ct) * 64 + l) * 8, 0, SC1);
        const u32x2 ev = {as_u(v.re), as_u(v.im)};
        if (g == 3) __builtin_amdgcn_raw_buffer_store_b64(ev, rws, WS_E64 + ((ct * 32 + rt) * 16 + r) * 8, 0, SC1);
      }
    }
    STAMP(22);
    WAIT_VM(0);
    if (l == 0) flag_st(a.ws + WS_FAS + ((2 * jb + wn) * 32 + rt) * 8, tag);
  }
  STAMP(3);
}

DN void p2_gemm(const Args& a, char* sm, int rt, int cg, int w, int l) {
  const int kf0 = kf_start(w), nf = w < 2 ? NF : NF - 1;
  const rsrc_t raf = mk_rsrc(a.ws + WS_H1F + (rt * KSF + kf0) * 4096, nf * 4096);
  const rsrc_t rbf_ = mk_rsrc(a.ws + WS_W2F + (cg * KSF + kf0) * 5120, nf * 5120);
  const rsrc_t ray = mk_rsrc(a.ws + WS_HF + (rt * KSY + NY * w) * 4096, NY * 4096);
  const rsrc_t rby = mk_rsrc(a.ws + WS_CF + (cg * KSY + NY * w) * 5120, NY * 5120);
  f32x4 acc[5][4];
  bf16x8 fa[PD][4], fb[PD][5];
  const int vo = l * 16;
  char* wreg = sm + w * WREG;
  auto from_lds = [](int k) NOPK { return k < 12 && (k & 1); };
  auto ld_b = [&](int k, int j) NOPK {
    if (from_lds(k)) {
      fb[k % PD][j] = *(const bf16x8*)(wreg + (k >> 1) * 5120 + j * 1024 + vo);
    } else {
      const int so = (k < NF ? k : k - NF) * 5120 + (j >> 2) * 4096;
      fb[k % PD][j] = __builtin_bit_cast(
          bf16x8, __builtin_amdgcn_raw_buffer_load_b128(k < NF ? rbf_ : rby, vo + (j & 3) * 1024, so, SC1));
    }
  };
  auto ld_a = [&](int k) NOPK {
#pragma unroll
    for (int i = 0; i < 4; ++i)
      fa[k % PD][i] = __builtin_bit_cast(
          bf16x8, __builtin_amdgcn_raw_buffer_load_b128(k < NF ? raf : ray, vo + i * 1024, (k < NF ? k : k - NF) * 4096, SC1));
  };
  char* red = wreg + (l & 15) * RED_ROW + (l >> 4) * 16;
  auto dump = [&]() NOPK {
#pragma unroll
    for (int j = 0; j < 5; ++j)
#pragma unroll
      for (int i = 0; i < 4; ++i) *(f32x4*)(red + i * 16 * RED_ROW + j * 64) = acc[j][i];
  };
  lds_wait2<1, 0>(lds_addr(sm + SY_PTOK), 0u, 0u);
#pragma unroll
  for (int k = 0; k < PD; ++k)
    if (!from_lds(k)) {
#pragma unroll
      for (int j = 0; j < 5; ++j) ld_b(k, j);
    }
  SB();
  lds_wait2<1, 4>(lds_addr(sm + SY_P2GO), 0u, 0u);
  lds_wait2<1, 0>(lds_addr(sm + SY_P2GO + 8), 0u, 0u);
  SB();
  STAMP(4);
#pragma unroll
  for (int k = 0; k < PD; ++k) {
    ld_a(k);
    if (from_lds(k)) {
#pragma unroll
      for (int j = 0; j < 5; ++j) ld_b(k, j);
    }
  }
  SB();
  const f32x4 z4 = {0.f, 0.f, 0.f, 0.f};
  unsigned yr = 0u;
#pragma unroll
  for (int k = 0; k < NF + NY; ++k) {
    const bool first = (k == 0 || k == NF);
#pragma unroll
    for (int j = 0; j < 5; ++j) {
#pragma unroll
      for (int i = 0; i < 4; ++i) acc[j][i] = MFMA(fb[k % PD][j], fa[k % PD][i], first ? z4 : acc[j][i]);
      SB();
      if (k + PD < NF + NY) ld_b(k + PD, j);
      SB();
    }
    if (k + PD == NF - 1) yr = lds_ld<unsigned>(sm + SY_YRDY);
    if (k + PD == NF) {
      STAMP(5);
      lds_wait2<1, 0>(lds_addr(sm + SY_YRDY), yr, yr);
      STAMP(6);
    }
    if (k + PD < NF + NY) ld_a(k + PD);
    SB();
    if (k == NF - 1) {
      dump();
      bar();
      STAMP(7);
    }
  }
  STAMP(8);
  bar();
  dump();
  bar();
  STAMP(9);
}

struct Early { u32x2 xv[5], dv[5]; u16 ldt[2], lre[2], lim[2]; };
DN void early_loads(const Args& a, int rt, int cg, int hw, int l, Early& y) {
#pragma unroll
  for (int e = 0; e < 5; ++e) {
    const int idx = hw * 64 + l + 256 * e, row = idx / 20, c4 = idx - row * 20;
    y.xv[e] = *(const u32x2*)(a.x + (size_t)(rt * 64 + row) * D_ + cg * 80 + c4 * 4);
    y.dv[e] = *(const u32x2*)(a.dsk + cg * 80 + c4 * 4);
  }
#pragma unroll
  for (int j = 0; j < 2; ++j) {
    const int p = 16 * (5 * cg + (j ? 4 : hw)) + (l & 15);
    y.ldt[j] = a.ldt[p]; y.lre[j] = a.lre[p]; y.lim[j] = a.lim[p];
  }
}
template <bool BU>
DN void p1_loader(const Args& a, char* sm, int ti, int tj, int rt, int cg, int lw, int l, Early& y) {
  const int l3 = l >> 3, l7 = l & 7, jb = tj - 9;
  const rsrc_t rx = mk_rsrc(a.x + (size_t)ti * 128 * D_, 128 * D_ * 2);
  const int vx = (BU ? (16 * (l3 >> 2) + (l3 & 3)) : l3) * (D_ * 2) + ((l7 ^ l3) << 4);
  rsrc_t rw0, rw1;
  int vw0, vw1 = 0, qmax = 5;
  if (BU) {
    rw0 = mk_rsrc(a.bre, P_ * D_ * 2);
    rw1 = mk_rsrc(a.bim, P_ * D_ * 2);
    vw0 = (96 * jb + l3) * (D_ * 2) + ((l7 ^ l3) << 4);
    qmax = (jb == 6) ? 3 : 5;
  } else {
    rw0 = mk_rsrc(a.w1, D_ * F_ * 2);
    rw1 = rw0;
    const int f0 = (l3 >> 1) & 1;
    const int base = l3 * (F_ * 2) + tj * 384 + (l7 & 1) * 16;
    vw0 = base + (((l7 >> 1) ^ f0) << 5);
    vw1 = base + (((l7 >> 1) ^ (f0 | 2)) << 5);
  }
  auto stage = [&](int s, int slot_i) NOPK {
    char* slot = sm + slot_i * SLOT;
#pragma unroll
    for (int v = 0; v < 4; ++v) {
#pragma unroll
      for (int q = 0; q < 4; ++q) {
        const int rowoff = BU ? (64 * (q >> 1) + 8 * (q & 1) + 32 * (v & 1) + 4 * (v >> 1)) : (32 * q + 8 * v);
        glds16<0>(rx, slot + (4 * q + v) * 1024, vx, rowoff * (D_ * 2) + s * 128);
      }
#pragma unroll
      for (int q = 0; q < 6; ++q) {
        if (BU) {
          const int qe = q < qmax ? q : qmax;
          glds16<0>(v < 2 ? rw0 : rw1, slot + 16384 + (4 * q + v) * 1024, vw0, (qe * 16 + 8 * (v & 1)) * (D_ * 2) + s * 128);
        } else {
          glds16<0>(rw0, slot + 16384 + (6 * v + q) * 1024, q < 3 ? vw0 : vw1,
                    (s * 64 + 16 * v + 8 * (q / 3)) * (F_ * 2) + (q % 3) * 128);
        }
      }
    }
  };
  const rsrc_t rb = mk_rsrc(a.ws + WS_W2F + cg * KSF * 5120, KSF * 5120);
  auto prefetch = [&](int slot_i) NOPK {
#pragma unroll
    for (int e = 0; e < 4; ++e) {
      const int gi = slot_i * 8 + lw * 4 + e, gw = gi / 6, gq = gi - 6 * gw;
      const int so = (kf_start(gw) + 2 * gq + 1) * 5120;
#pragma unroll
      for (int j = 0; j < 5; ++j) glds16<SC1>(rb, sm + gi * 5120 + j * 1024, l * 16, so + j * 1024);
    }
  };
  bar();
  __builtin_amdgcn_s_setprio(3);
  int slot_i = lw;
#pragma unroll 1
  for (int t = lw; t < 10; t += 2) {
    if (t >= 3) lds_wait(sm + SY_FREE, t - 2, lds_ld<u32x4>(sm + SY_FREE));
    stage(t, slot_i);
    WAIT_VM(0);
    lds_st(sm + SY_FULL + 4 * lw, t + 1);
    slot_i = slot_i == 0 ? 2 : slot_i - 1;
  }
  early_loads(a, rt, cg, lw, l, y);
  SB();
  lds_wait(sm + SY_PTOK, 1u, lds_ld<unsigned>(sm + SY_PTOK));
  lds_wait(sm + SY_FREE, 8u, lds_ld<u32x4>(sm + SY_FREE));
  prefetch(1);
  lds_wait(sm + SY_FREE, 9u, lds_ld<u32x4>(sm + SY_FREE));
  prefetch(2);
  lds_wait(sm + SY_FREE, 10u, lds_ld<u32x4>(sm + SY_FREE));
  prefetch(0);
}

template <bool BU>
DN void p1_side(const Args& a, char* sm, int ti, int tj, int rt, int cg, int sw, int l) {
  const int jb = tj - 9, r = l & 15, g = l >> 4, l3 = l >> 3, l7 = l & 7;
  const int bl = ti * 16 + tj, pcg = bl & 7, pq = bl >> 3;
  const rsrc_t rws = mk_rsrc(a.ws, WS_END);
  if (sw == 0 && l < 32) lds_st(sm + LD_SYNC + 4 * l, l == 19 ? 1u : 0u);
  bar();
  unsigned tv[10];
#pragma unroll
  for (int i = 0; i < 10; ++i) tv[i] = 0u;
  SB();
  u32x4 va[20];
  {
    const char *p0, *p1;
    int step;
    if (sw == 0) {
      p0 = (const char*)(a.x + (size_t)(ti * 128 + 8 * tj + l3) * D_) + l7 * 16;
      p1 = p0;
      step = 128;
    } else if (BU) {
      const char* pp[2];
#pragma unroll
      for (int h = 0; h < 2; ++h) {
        int n = 12 * ti + 8 * h + l3;
        n = (8 * h + l3 < 12) ? n : 12 * ti;
        int ch = 96 * jb + 16 * (n >> 5) + (n & 15);
        ch = ch < P_ ? ch : P_ - 1;
        pp[h] = (const char*)(((n >> 4) & 1 ? a.bim : a.bre) + (size_t)ch * D_) + l7 * 16;
      }
      p0 = pp[0]; p1 = pp[1];
      step = 128;
    } else {
      const int lc = l < 48 ? l : l - 48, c = lc % 24, rg = lc / 24;
      p0 = (const char*)(a.w1 + (size_t)(40 * ti + 20 * rg) * F_ + 192 * tj) + c * 16;
      step = F_ * 2;
      p1 = p0 + 10 * step;
    }
#pragma unroll
    for (int i = 0; i < 10; ++i) va[i] = *(const u32x4*)(p0 + i * step);
    if (sw) {
#pragma unroll
      for (int i = 0; i < 10; ++i) va[10 + i] = *(const u32x4*)(p1 + i * step);
    }
  }
  const u64 ep = flag_ld(a.ws + WS_EP);
  u16 c_ldt = 0, c_lre = 0, c_lim = 0;
  if (BU) {
    int p = 96 * jb + 48 * sw + (l < 48 ? l : 0);
    p = p < P_ ? p : P_ - 1;
    c_ldt = a.ldt[p]; c_lre = a.lre[p]; c_lim = a.lim[p];
  }
  SB();
  u32x4 vc[10];
  const int cks0 = 3 * (pq - 18), cfr = pq < 31 ? 15 : 5;
  auto piece = [&](int e) NOPK {
    const int pc = sw ? 10 + (e % 5) : e;
    return pc < cfr ? pc : cfr - 1;
  };
  if (pq < 18) {
    const rsrc_t r2 = mk_rsrc(a.w2, F_ * D_ * 2);
#pragma unroll
    for (int e = 0; e < 10; ++e) {
      const int pc = piece(e);
      const int q = pc * 64 + l, row = q / 10, ch = q - row * 10;
      if (e < 5 || sw == 0)
        glds16<0>(r2, sm + LD_AUX + pc * 1024, (96 * pq + row) * (D_ * 2) + pcg * 160 + ch * 16, 0);
    }
  } else {
    const u16* cm = g < 2 ? a.cre : a.cim;
#pragma unroll
    for (int e = 0; e < 10; ++e) {
      const int pc = piece(e), ksl = pc / 5, nt = pc - 5 * ksl;
      glds16_g(cm + (size_t)(80 * pcg + 16 * nt + r) * P_ + 16 * (cks0 + ksl) + 8 * (g & 1), sm + LD_AUX + pc * 1024,
               e < 5 || sw == 0);
    }
  }
  SB();
  WAIT_VM(0);
#pragma unroll
  for (int i = 0; i < 10; ++i) asm volatile("" ::"v"(tv[i]));
  if (sw == 0) {
    float s = 0.f;
#pragma unroll
    for (int i = 0; i < 10; ++i) s += sum8(va[i]);
    const float mean = red8(s, l) * (1.f / D_);
    float q = 0.f;
#pragma unroll
    for (int i = 0; i < 10; ++i)
#pragma unroll
      for (int e = 0; e < 4; ++e) {
        const float d0 = lo_f(va[i][e]) - mean, d1 = hi_f(va[i][e]) - mean;
        q = __builtin_fmaf(d0, d0, __builtin_fmaf(d1, d1, q));
      }
    const float var = red8(q, l) * (1.f / D_);
    const float sd = rbf(__builtin_sqrtf(rbf(rbf(var) + 1e-5f)));
    const u32x2 o = {as_u(rbf(mean)), as_u(sd)};
    if (l7 == 0) __builtin_amdgcn_raw_buffer_store_b64(o, rws, WS_STAT + (ti * 128 + 8 * tj + l3) * 8, 0, SC1);
    if (l == 0) *(u64*)(sm + SY_TAG) = ep + 0x9E3779B97F4A7C15ull;
    lds_st(sm + SY_TAG + 8, 1u);
  } else if (BU) {
#pragma unroll
    for (int h = 0; h < 2; ++h) {
      float s = 0.f;
#pragma unroll
      for (int i = 0; i < 10; ++i) s += sum8(va[10 * h + i]);
      s = red8(s, l);
      if (l7 == 0 && 8 * h + l3 < 12)
        __builtin_amdgcn_raw_buffer_store_b32(as_u(s), rws, WS_CSS + (jb * 192 + 12 * ti + 8 * h + l3) * 4, 0, SC1);
    }
  } else {
    float s[8];
#pragma unroll
    for (int e = 0; e < 8; ++e) s[e] = 0.f;
#pragma unroll
    for (int i = 0; i < 20; ++i)
#pragma unroll
      for (int e = 0; e < 4; ++e) { s[2 * e] += lo_f(va[i][e]); s[2 * e + 1] += hi_f(va[i][e]); }
#pragma unroll
    for (int e = 0; e < 8; ++e) s[e] += lane_get(s[e], l + 24);
    if (l < 24) {
#pragma unroll
      for (int h = 0; h < 2; ++h) {
        const u32x4 o = {as_u(s[4 * h]), as_u(s[4 * h + 1]), as_u(s[4 * h + 2]), as_u(s[4 * h + 3])};
        __builtin_amdgcn_raw_buffer_store_b128(o, rws, WS_CSF + ((tj * 16 + ti) * 192 + l * 8 + 4 * h) * 4, 0, SC1);
      }
    }
  }
  STAMP(12);
  if (pq < 18) {
#pragma unroll
    for (int e = 0; e < 10; ++e) {
      const int pc = piece(e), ksl = pc / 5, nt = pc - 5 * ksl;
      const char* b = sm + LD_AUX + (32 * ksl + 4 * g + (r >> 2)) * 160 + nt * 32 + (r & 3) * 8;
      vc[e] = __builtin_bit_cast(u32x4, tr_frag(b, b + 16 * 160));
    }
  } else {
#pragma unroll
    for (int e = 0; e < 10; ++e) vc[e] = *(const u32x4*)(sm + LD_AUX + piece(e) * 1024 + l * 16);
  }
  {
    const unsigned dst = pq < 18 ? WS_W2F + (pcg * KSF + 3 * pq) * 5120 : WS_CF + (pcg * KSY + cks0) * 5120;
#pragma unroll
    for (int e = 0; e < 10; ++e)
      if (e < 5 || sw == 0)
        __builtin_amdgcn_raw_buffer_store_b128(vc[e], rws, dst + piece(e) * 1024 + l * 16, 0, SC1);
  }
  if (BU) {
    cpx lam, coef;
    chan_consts(c_ldt, c_lre, c_lim, lam, coef);
    if (l < 48) *(f32x2*)(sm + LD_LAM + (48 * sw + l) * 8) = (f32x2){lam.re, lam.im};
  }
  lds_wait(sm + SY_TAG + 8, 1u, lds_ld<unsigned>(sm + SY_TAG + 8));
  const u64 tag = *(const u64*)(sm + SY_TAG);
  WAIT_VM(0);
  if (l < 2)
    flag_st(a.ws + (l == 1 ? WS_FPT + ((pcg * 32 + pq) * 2 + sw) * 8
                           : (sw == 0 ? WS_FST + (ti * 16 + tj) * 8 : WS_FCS + (tj * 16 + ti) * 8)), tag);
  STAMP(13);
  const char* fp = a.ws + (sw == 0 ? WS_FST + (l < 16 ? ti : (rt >> 1)) * 128 : WS_FCS + tj * 128) + r * 8;
  lds_wait(sm + SY_FREE, GATHER_AT, lds_ld<unsigned>(sm + SY_FREE));
  flag_wait(fp, l < (sw == 0 ? 32 : 16), tag);
  u32x4 gv[12];
  if (sw == 0) {
    gv[0] = __builtin_amdgcn_raw_buffer_load_b128(rws, WS_STAT + ti * 1024 + l * 16, 0, SC1);
    const u32x2 s2 = __builtin_amdgcn_raw_buffer_load_b64(rws, WS_STAT + rt * 512 + l * 8, 0, SC1);
    *(u32x2*)(sm + LD_ST2 + l * 8) = s2;
#pragma unroll
    for (int h = 0; h < 2; ++h) {
      const float mb = as_f(gv[0][2 * h]), sd = as_f(gv[0][2 * h + 1]);
      const float ri = 1.f / sd;
      ((float*)(sm + LD_RM))[2 * l + h] = BU ? mb * ri : mb;
      ((float*)(sm + LD_RI))[2 * l + h] = ri;
    }
  } else if (BU) {
    gv[0] = __builtin_amdgcn_raw_buffer_load_b128(rws, WS_CSS + jb * 768 + (l < 48 ? l : 0) * 16, 0, SC1);
    if (l < 48) *(u32x4*)(sm + LD_CS + l * 16) = gv[0];
  } else {
#pragma unroll
    for (int i = 0; i < 12; ++i)
      gv[i] = __builtin_amdgcn_raw_buffer_load_b128(
          rws, WS_CSF + (tj * 16 + 4 * (i / 3) + g) * 768 + (r + 16 * (i % 3)) * 16, 0, SC1);
#pragma unroll
    for (int c = 0; c < 3; ++c) {
      f32x4 s;
#pragma unroll
      for (int e = 0; e < 4; ++e) {
        float t = (as_f(gv[c][e]) + as_f(gv[3 + c][e])) + (as_f(gv[6 + c][e]) + as_f(gv[9 + c][e]));
        t += lane_get(t, l ^ 16);
        t += lane_get(t, l ^ 32);
        s[e] = t;
      }
      if (g == 0) *(f32x4*)(sm + LD_CS + (r + 16 * c) * 16) = s;
    }
  }
  asm volatile("" ::: "memory");
  lds_st(sm + SY_GATH + 4 * sw, 1u);
  STAMP(14);
  if (sw == 0) {
    const char* fpt = a.ws + WS_FPT + cg * 512 + l * 8;
    flag_wait(fpt, true, tag);
    lds_st(sm + SY_PTOK, 1u);
  }
  STAMP(15);
}

struct JobC { cpx l1, cf, l256, fac, lg, cl; };
struct JobD { u32x2 e[8], pre; u32x4 h[4]; };
DN void job_consts(u16 ldt, u16 lre, u16 lim, int rt, int l, JobC& k) {
  const int g = l >> 4;
  cpx cf;
  chan_consts(ldt, lre, lim, k.l1, cf);
  k.cf = {2.f * cf.re, 2.f * cf.im};
  const cpx l2 = c_mul(k.l1, k.l1), l4 = c_mul(l2, l2), l8 = c_mul(l4, l4), l16 = c_mul(l8, l8), l32 = c_mul(l16, l16),
            l64 = c_mul(l32, l32), l128 = c_mul(l64, l64);
  k.l256 = c_mul(l128, l128);
  const int ex = (rt - 1 - g) & 3;
  const cpx one = {1.f, 0.f};
  k.fac = c_mul(c_sel(ex & 1, l64, one), c_sel(ex & 2, l128, one));
  k.lg = c_mul(c_sel(g & 1, l16, one), c_sel(g & 2, l32, one));
  k.cl = c_mul(k.cf, k.l1);
  asm volatile("" : "+v"(k.l1.re), "+v"(k.l1.im), "+v"(k.cf.re), "+v"(k.cf.im), "+v"(k.l256.re), "+v"(k.l256.im),
                    "+v"(k.fac.re), "+v"(k.fac.im), "+v"(k.lg.re), "+v"(k.lg.im), "+v"(k.cl.re), "+v"(k.cl.im));
}
DN void job_load(rsrc_t rws, int rt, int ct, int l, JobD& d) {
  const int c = l & 15, g = l >> 4;
#pragma unroll
  for (int i = 0; i < 8; ++i)
    d.e[i] = __builtin_amdgcn_raw_buffer_load_b64(rws, WS_E64 + ((ct * 32 + 4 * i + g) * 16 + c) * 8, 0, SC1);
  d.pre = __builtin_amdgcn_raw_buffer_load_b64(rws, WS_PPRE + ((rt * KSY + ct) * 64 + l) * 8, 0, SC1);
#pragma unroll
  for (int q = 0; q < 4; ++q)
    d.h[q] = __builtin_amdgcn_raw_buffer_load_b128(rws, WS_HLOC + ((rt * KSY + ct) * 4 + q) * 1024 + l * 16, 0, SC1);
}
DN void job_run(rsrc_t rws, char* scr, int rt, int ct, int l, const JobC& k, const JobD& d) {
  const int c = l & 15, g = l >> 4;
  cpx H = {0.f, 0.f};
#pragma unroll
  for (int i = 0; i < 8; ++i) H = c_sel(4 * i + g < rt, c_fma(k.l256, H, cpx{as_f(d.e[i][0]), as_f(d.e[i][1])}), H);
  cpx cr = c_mul(k.fac, H);
  cr.re += lane_get(cr.re, l ^ 16); cr.im += lane_get(cr.im, l ^ 16);
  cr.re += lane_get(cr.re, l ^ 32); cr.im += lane_get(cr.im, l ^ 32);
  const cpx carry = c_fma(k.lg, cr, cpx{as_f(d.pre[0]), as_f(d.pre[1])});
  cpx G = c_mul(k.cl, carry);
  u32x4 ore[2], oim[2];
#pragma unroll
  for (int q = 0; q < 2; ++q)
#pragma unroll
    for (int i = 0; i < 4; ++i) {
      const cpx a0 = c_fma(k.cf, cpx{lo_f(d.h[q][i]), lo_f(d.h[2 + q][i])}, G);
      G = c_mul(k.l1, G);
      const cpx a1 = c_fma(k.cf, cpx{hi_f(d.h[q][i]), hi_f(d.h[2 + q][i])}, G);
      G = c_mul(k.l1, G);
      ore[q][i] = pk_bf16(a0.re, a1.re);
      oim[q][i] = pk_bf16(-a0.im, -a1.im);
    }
#pragma unroll
  for (int q = 0; q < 2; ++q) {
    *(u32x4*)(scr + c * 144 + g * 32 + q * 16) = ore[q];
    *(u32x4*)(scr + (16 + c) * 144 + g * 32 + q * 16) = oim[q];
  }
  const char* b = scr + (8 * g + (c >> 2)) * 144 + (c & 3) * 8;
  u32x4 v[4];
#pragma unroll
  for (int mt = 0; mt < 4; ++mt) v[mt] = __builtin_bit_cast(u32x4, tr_frag(b + mt * 32, b + mt * 32 + 4 * 144));
#pragma unroll
  for (int mt = 0; mt < 4; ++mt)
    __builtin_amdgcn_raw_buffer_store_b128(v[mt], rws, WS_HF + ((rt * KSY + ct) * 4 + mt) * 1024 + l * 16, 0, SC1);
}

DN void p2_helper(const Args& a, char* sm, int rt, int cg, int hw, int l, const Early& y) {
  const u64 tag = *(const u64*)(sm + SY_TAG);
  const rsrc_t rws = mk_rsrc(a.ws, WS_END);
  const int ht = hw * 64 + l;
  const int ct0 = 5 * cg + hw, ct1 = 5 * cg + 4;
  JobC k0, k1;
  job_consts(y.ldt[0], y.lre[0], y.lim[0], rt, l, k0);
  if (hw == 3) job_consts(y.ldt[1], y.lre[1], y.lim[1], rt, l, k1);
  if (hw < 2) {
    WAIT_VM(0);
    lds_st(sm + SY_P2GO + 4 * hw, 1u);
  }
  lds_wait(sm + SY_GATH, 1u, lds_ld<unsigned>(sm + SY_GATH));
  float xs[5][4], xd[5][4], gt[5][4], ri[5];
#pragma unroll
  for (int e = 0; e < 5; ++e) {
    const int row = (ht + 256 * e) / 20;
    const f32x2 st = *(const f32x2*)(sm + LD_ST2 + row * 8);
    xs[e][0] = lo_f(y.xv[e][0]); xs[e][1] = hi_f(y.xv[e][0]); xs[e][2] = lo_f(y.xv[e][1]); xs[e][3] = hi_f(y.xv[e][1]);
    const float ds[4] = {lo_f(y.dv[e][0]), hi_f(y.dv[e][0]), lo_f(y.dv[e][1]), hi_f(y.dv[e][1])};
    const float mb = st[0];
    ri[e] = 1.f / st[1];
#pragma unroll
    for (int i = 0; i < 4; ++i) xd[e][i] = rbf(rbf(rbf(xs[e][i] - mb) * ri[e]) * ds[i]);
    asm volatile("" : "+v"(xd[e][0]), "+v"(xd[e][1]), "+v"(xd[e][2]), "+v"(xd[e][3]), "+v"(ri[e]));
  }
  STAMP(3);
  lds_wait(sm + SY_FREE, 11u, lds_ld<unsigned>(sm + SY_FREE));
  if (hw == 2) {
    const char* fp = a.ws + WS_FAF + (rt * 32 + (l & 31)) * 8;
    flag_wait(fp, l < 18, tag);
    lds_st(sm + SY_P2GO + 8, 1u);
  }
  STAMP(4);
  {
    const int ctf = (l < 32 || hw != 3) ? ct0 : ct1;
    const char* fp = a.ws + WS_FAS + ((ctf / 3) * 32 + (l & 31)) * 8;
    flag_wait(fp, (l & 31) <= rt && (l < 32 || hw == 3), tag);
  }
  STAMP(16);
  JobD d0, d1;
  job_load(rws, rt, ct0, l, d0);
  if (hw == 3) job_load(rws, rt, ct1, l, d1);
  SB();
  job_run(rws, sm + LD_AUX + hw * 4608, rt, ct0, l, k0, d0);
  if (hw == 3) job_run(rws, sm + LD_AUX + hw * 4608, rt, ct1, l, k1, d1);
  STAMP(17);
  WAIT_VM(0);
  if (l == 0) flag_st(a.ws + WS_FH + (rt * 32 + cg * 4 + hw) * 8, tag);
  STAMP(5);
  if (hw == 0) {
    const char* fp = a.ws + WS_FH + (rt * 32 + (l & 31)) * 8;
    flag_wait(fp, l < 32, tag);
    lds_st(sm + SY_YRDY, 1u);
  }
  if (blockIdx.x == 0 && hw == 1) {
#pragma unroll
    for (int i = 0; i < 4; ++i) {
      const char* fp = a.ws + WS_FST + (i * 64 + l) * 8;
      flag_wait(fp, true, tag);
    }
    if (l == 0) flag_st(a.ws + WS_EP, tag);
  }
  STAMP(6);
  bar();
  STAMP(7);
#pragma unroll
  for (int e = 0; e < 5; ++e) {
    const int idx = ht + 256 * e, row = idx / 20, c4 = idx - row * 20;
    const char* p = sm + row * RED_ROW + c4 * 16;
    const f32x4 p0 = *(const f32x4*)p, p1 = *(const f32x4*)(p + WREG), p2 = *(const f32x4*)(p + 2 * WREG),
                p3 = *(const f32x4*)(p + 3 * WREG);
#pragma unroll
    for (int i = 0; i < 4; ++i) {
      const float fv = rbf(((p0[i] + p1[i]) + (p2[i] + p3[i])) * ri[e]);
      gt[e][i] = rbf(fv / (1.f + __builtin_amdgcn_exp2f(-1.44269504088896f * fv)));
    }
    asm volatile("" : "+v"(gt[e][0]), "+v"(gt[e][1]), "+v"(gt[e][2]), "+v"(gt[e][3]));
  }
  STAMP(8);
  bar();
  bar();
  STAMP(9);
#pragma unroll
  for (int e = 0; e < 5; ++e) {
    const int idx = ht + 256 * e, row = idx / 20, c4 = idx - row * 20;
    const char* p = sm + row * RED_ROW + c4 * 16;
    const f32x4 p0 = *(const f32x4*)p, p1 = *(const f32x4*)(p + WREG), p2 = *(const f32x4*)(p + 2 * WREG),
                p3 = *(const f32x4*)(p + 3 * WREG);
    f32x4 o4;
#pragma unroll
    for (int i = 0; i < 4; ++i) o4[i] = xs[e][i] + (((p0[i] + p1[i]) + (p2[i] + p3[i])) + xd[e][i]) * gt[e][i];
    *(f32x4*)(a.out + (size_t)(rt * 64 + row) * D_ + cg * 80 + c4 * 4) = o4;
  }
}

__global__ __launch_bounds__(512, 1) NOPK void fused_kernel(Args a) {
  extern __shared__ __attribute__((aligned(16))) char sm[];
  const int tid = threadIdx.x, l = tid & 63;
  const int w = __builtin_amdgcn_readfirstlane(tid >> 6);
  const int b = blockIdx.x, x = b & 7, o = b >> 3;
  const int ti = (x & 1) * 8 + (o & 7);
  const int tj = (x >> 1) * 4 + (o >> 3);
  const int rt = (x >> 1) * 8 + (o >> 2);
  const int cg = (x & 1) * 4 + (o & 3);
  STAMP(0);
  const u64 pe0 = __builtin_amdgcn_s_memrealtime();
  if (w < 4) {
    __builtin_amdgcn_s_setprio(1);
    if (tj < 9) p1_gemm<false>(a, sm, ti, tj, w, l);
    else p1_gemm<true>(a, sm, ti, tj, w, l);
    p2_gemm(a, sm, rt, cg, w, l);
  } else {
    Early y;
    if (w < 6) {
      if (tj < 9) p1_loader<false>(a, sm, ti, tj, rt, cg, w - 4, l, y);
      else p1_loader<true>(a, sm, ti, tj, rt, cg, w - 4, l, y);
    } else {
      if (tj < 9) p1_side<false>(a, sm, ti, tj, rt, cg, w - 6, l);
      else p1_side<true>(a, sm, ti, tj, rt, cg, w - 6, l);
      early_loads(a, rt, cg, w - 4, l, y);
    }
    STAMP(2);
    __builtin_amdgcn_s_setprio(2);
    p2_helper(a, sm, rt, cg, w - 4, l, y);
  }
  while (__builtin_amdgcn_s_memrealtime() - pe0 < PROBE2) __builtin_amdgcn_s_sleep(1);
  STAMP(10);
  STAMP(11);
}

extern "C" void kernel_launch(void* const* d_in, const int* in_sizes, int n_in, void* d_out, int out_size,
                              void* d_ws, size_t ws_size, hipStream_t stream) {
  (void)in_sizes; (void)n_in; (void)out_size; (void)ws_size;
  Args a;
  a.x = (const u16*)d_in[0]; a.lre = (const u16*)d_in[1]; a.lim = (const u16*)d_in[2];
  a.bre = (const u16*)d_in[3]; a.bim = (const u16*)d_in[4]; a.cre = (const u16*)d_in[5];
  a.cim = (const u16*)d_in[6]; a.ldt = (const u16*)d_in[7]; a.dsk = (const u16*)d_in[8];
  a.w1 = (const u16*)d_in[10]; a.w2 = (const u16*)d_in[11];
  a.ws = (char*)d_ws; a.out = (float*)d_out;
  (void)hipFuncSetAttribute((const void*)fused_kernel, hipFuncAttributeMaxDynamicSharedMemorySize, LDS_BYTES + 512);
  fused_kernel<<<256, 512, LDS_BYTES + 512, stream>>>(a);
}
```

```cpp
#include <hip/hip_runtime.h>
#include <stdint.h>

typedef unsigned short u16;
typedef unsigned long long u64;
typedef __attribute__((ext_vector_type(8))) short bf16x8;
typedef __attribute__((ext_vector_type(4))) short s16x4;
typedef __attribute__((ext_vector_type(4))) float f32x4;
typedef __attribute__((ext_vector_type(2))) float f32x2;
typedef __attribute__((ext_vector_type(4))) unsigned u32x4;
typedef __attribute__((ext_vector_type(2))) unsigned u32x2;
typedef __attribute__((ext_vector_type(2))) __bf16 bf16x2;
typedef __amdgpu_buffer_rsrc_t rsrc_t;
#define LDS_AS __attribute__((address_space(3)))
#define NOPK
#define DN __device__ __forceinline__ NOPK
#define MFMA(a, b, c) __builtin_amdgcn_mfma_f32_16x16x32_bf16((a), (b), (c), 0, 0, 0)
#define SB() __builtin_amdgcn_sched_barrier(0)
#define WAIT_VM(n) asm volatile("s_waitcnt vmcnt(" #n ")" ::: "memory")
#define SC1 16
template <int V> struct IC { static constexpr int value = V; };

constexpr int L_ = 2048, D_ = 640, P_ = 640, F_ = 1728;
constexpr int KSF = F_ / 32;
constexpr int KSY = P_ / 16;

constexpr unsigned WS_H1F = 0;
constexpr unsigned WS_HF = WS_H1F + 32u * KSF * 4096;
constexpr unsigned WS_W2F = WS_HF + 32u * KSY * 4096;
constexpr unsigned WS_CF = WS_W2F + 8u * KSF * 5120;
constexpr unsigned WS_E64 = WS_CF + 8u * KSY * 5120;
constexpr unsigned WS_STAT = WS_E64 + KSY * 32u * 256;
constexpr unsigned WS_CSF = WS_STAT + L_ * 8u;
constexpr unsigned WS_CSS = WS_CSF + 9u * 16 * 768;
constexpr unsigned WS_FST = WS_CSS + 7u * 768 + 768;
constexpr unsigned WS_FCS = WS_FST + 2048;
constexpr unsigned WS_FPT = WS_FCS + 2048;
constexpr unsigned WS_FAF = WS_FPT + 4096;
constexpr unsigned WS_FAS = WS_FAF + 8192;
constexpr unsigned WS_FH = WS_FAS + 4096;
constexpr unsigned WS_EP = WS_FH + 8192;
constexpr unsigned WS_END = WS_EP + 128;

constexpr int SLOT = 40960;
constexpr int WREG = 21504;
constexpr int LD_P2 = SLOT;
constexpr int LD_AUX = LD_P2 + 4 * WREG;
constexpr int JOBSZ = 5120;
constexpr int LD_RM = LD_AUX + 20480;
constexpr int LD_RI = LD_RM + 512;
constexpr int LD_CS = LD_RI + 512;
constexpr int LD_LAM = LD_CS + 768;
constexpr int LD_CAR = LD_LAM + 4608;
constexpr int LD_ST2 = LD_CAR + 1536;
constexpr int LD_SYNC = LD_ST2 + 512;
constexpr int LDS_BYTES = LD_SYNC + 128;
constexpr int SY_TAG = LD_SYNC, SY_FULL = LD_SYNC + 16, SY_FREE = LD_SYNC + 32, SY_GATH = LD_SYNC + 48,
              SY_PTOK = LD_SYNC + 56, SY_P2GO = LD_SYNC + 64, SY_YRDY = LD_SYNC + 80, SY_CAR = LD_SYNC + 96;
constexpr int RED_ROW = 336;
constexpr int PD = 3, NF = 14, NY = 10;
#define GATHER_AT 7u
#define YPOLL_AT 6
#define E64_DELAY 8
#define FAF_DELAY 8
constexpr unsigned SPIN_MAX = 1u << 18;

#define STAMP(i)

struct Args {
  const u16 *x, *lre, *lim, *bre, *bim, *cre, *cim, *ldt, *dsk, *w1, *w2;
  char* ws;
  float* out;
};

struct cpx { float re, im; };
DN float fma_(float a, float b, float c) {
  float d;
  asm("v_fma_f32 %0, %1, %2, %3" : "=v"(d) : "v"(a), "v"(b), "v"(c));
  return d;
}
DN float fnma_(float a, float b, float c) {
  float d;
  asm("v_fma_f32 %0, -%1, %2, %3" : "=v"(d) : "v"(a), "v"(b), "v"(c));
  return d;
}
DN float mul_(float a, float b) {
  float d;
  asm("v_mul_f32 %0, %1, %2" : "=v"(d) : "v"(a), "v"(b));
  return d;
}
DN cpx c_mul(cpx a, cpx b) { return {fnma_(a.im, b.im, mul_(a.re, b.re)), fma_(a.im, b.re, mul_(a.re, b.im))}; }
DN cpx c_fma(cpx a, cpx b, cpx c) {
  return {fma_(a.re, b.re, fnma_(a.im, b.im, c.re)), fma_(a.re, b.im, fma_(a.im, b.re, c.im))};
}
DN cpx c_sel(bool p, cpx a, cpx b) { return {p ? a.re : b.re, p ? a.im : b.im}; }
DN float lane_get(float v, int src) {
  return __builtin_bit_cast(float, __builtin_amdgcn_ds_bpermute(src << 2, __builtin_bit_cast(int, v)));
}
DN unsigned pk_bf16(float lo, float hi) {
  f32x2 f = {lo, hi};
  return __builtin_bit_cast(unsigned, __builtin_convertvector(f, bf16x2));
}
DN float as_f(unsigned u) { return __builtin_bit_cast(float, u); }
DN unsigned as_u(float f) { return __builtin_bit_cast(unsigned, f); }
DN float lo_f(unsigned u) { return __builtin_bit_cast(float, u << 16); }
DN float hi_f(unsigned u) { return __builtin_bit_cast(float, u & 0xffff0000u); }
DN float bf2f(u16 v) { return lo_f(v); }
DN float rbf(float f) { return lo_f(pk_bf16(f, 0.f)); }
DN rsrc_t mk_rsrc(const void* p, unsigned bytes) {
  return __builtin_amdgcn_make_buffer_rsrc((void*)p, 0, bytes, 0x00020000);
}
template <int AUX>
DN void glds16(rsrc_t r, char* lds, int voff, int soff) {
  __builtin_amdgcn_raw_ptr_buffer_load_lds(r, (LDS_AS void*)lds, 16, voff, soff, 0, AUX);
}
#define BARS(i) bar()
DN void glds16_g(const void* src, char* lds, bool on) {
  if (on) __builtin_amdgcn_global_load_lds((const __attribute__((address_space(1))) void*)src, (LDS_AS void*)lds, 16, 0, 0);
}
DN void bar() {
  asm volatile("s_waitcnt lgkmcnt(0)" ::: "memory");
  __builtin_amdgcn_s_barrier();
  asm volatile("" ::: "memory");
  SB();
}
template <class T> DN T lds_ld(const char* p) { return *(volatile LDS_AS T*)(LDS_AS char*)p; }
DN void lds_st(char* p, unsigned v) {
  asm volatile("" ::: "memory");
  *(volatile LDS_AS unsigned*)(LDS_AS char*)p = v;
  asm volatile("" ::: "memory");
}
DN unsigned vmin(u32x2 v) { return v[0] < v[1] ? v[0] : v[1]; }
DN unsigned vmin(u32x4 v) {
  const unsigned a = v[0] < v[1] ? v[0] : v[1], b = v[2] < v[3] ? v[2] : v[3];
  return a < b ? a : b;
}
DN unsigned vmin(unsigned v) { return v; }
template <class T> DN void lds_wait(const char* p, unsigned n, T first) {
  unsigned spins = 0;
  while (__builtin_amdgcn_readfirstlane(vmin(first)) < n && spins < 64u * SPIN_MAX) {
    __builtin_amdgcn_s_sleep(1);
    first = lds_ld<T>(p);
    ++spins;
  }
  asm volatile("" ::: "memory");
}
template <int N, int OFF2>
DN void lds_wait2(unsigned addr, unsigned f0, unsigned f1) {
  unsigned s0, s1, cnt;
  asm volatile(
      "s_mov_b32 %4, 0\n"
      "1:\n\t"
      "v_readfirstlane_b32 %2, %0\n\t"
      "v_readfirstlane_b32 %3, %1\n\t"
      "s_add_u32 %4, %4, 1\n\t"
      "s_min_u32 %2, %2, %3\n\t"
      "s_cmp_ge_u32 %2, %6\n\t"
      "s_cbranch_scc1 2f\n\t"
      "s_bitcmp1_b32 %4, 24\n\t"
      "s_cbranch_scc1 2f\n\t"
      "s_sleep 1\n\t"
      "ds_read_b32 %0, %5\n\t"
      "ds_read_b32 %1, %5 offset:%7\n\t"
      "s_waitcnt lgkmcnt(0)\n\t"
      "s_branch 1b\n"
      "2:"
      : "+v"(f0), "+v"(f1), "=&s"(s0), "=&s"(s1), "=&s"(cnt)
      : "v"(addr), "i"(N), "i"(OFF2)
      : "scc", "memory");
}
DN unsigned lds_addr(const char* p) { return (unsigned)(uintptr_t)(LDS_AS const char*)p; }
DN bf16x8 tr_frag(const char* p0, const char* p1) {
  s16x4 lo = __builtin_amdgcn_ds_read_tr16_b64_v4i16((LDS_AS s16x4*)p0);
  s16x4 hi = __builtin_amdgcn_ds_read_tr16_b64_v4i16((LDS_AS s16x4*)p1);
  return __builtin_shufflevector(lo, hi, 0, 1, 2, 3, 4, 5, 6, 7);
}
DN u64 flag_ld(const char* p) { return __hip_atomic_load((const u64*)p, __ATOMIC_RELAXED, __HIP_MEMORY_SCOPE_AGENT); }
DN void flag_st(char* p, u64 v) { __hip_atomic_store((u64*)p, v, __ATOMIC_RELAXED, __HIP_MEMORY_SCOPE_AGENT); }
#define POLL_SLEEP 8
DN void flag_wait(const char* p, bool need, u64 tag) {
  unsigned spins = 0;
  for (;;) {
    if (need) need = flag_ld(p) != tag;
    if (__builtin_amdgcn_ballot_w64(need) == 0 || ++spins >= SPIN_MAX) break;
    __builtin_amdgcn_s_sleep(POLL_SLEEP);
  }
  __builtin_amdgcn_fence(__ATOMIC_ACQUIRE, "wavefront");
}
DN float exp_acc(float x) {
  const float l2e = 1.44269502162933349609375f, l2e_lo = 1.925963033500011e-08f;
  const float t = x * l2e;
  const float e = __builtin_fmaf(x, l2e_lo, __builtin_fmaf(x, l2e, -t));
  const float r = __builtin_amdgcn_exp2f(t);
  return __builtin_fmaf(r, e * 0.693147180559945f, r);
}
DN void sincos_acc(float x, float& s, float& c) {
  const float k = __builtin_rintf(x * 0.636619772367581f);
  float r = __builtin_fmaf(k, -1.57079601287841796875f, x);
  r = __builtin_fmaf(k, -3.1391647326017846353352069854736328125e-7f, r);
  r = __builtin_fmaf(k, -5.390302529957764765544681040410068817436695098876953125e-15f, r);
  const float z = r * r;
  float ps = __builtin_fmaf(z, 2.7183114939898219064e-6f, -1.98393348360966317347e-4f);
  ps = __builtin_fmaf(z, ps, 8.3333293858894631756e-3f);
  ps = __builtin_fmaf(z, ps, -1.66666666416265235595e-1f);
  const float sn = __builtin_fmaf(r * z, ps, r);
  float pc = __builtin_fmaf(z, 2.43904487962774090654e-5f, -1.38867637746099294692e-3f);
  pc = __builtin_fmaf(z, pc, 4.16666233237390631894e-2f);
  pc = __builtin_fmaf(z, pc, -4.99999997251031003120e-1f);
  const float cs = __builtin_fmaf(z, pc, 1.f);
  const int q = (int)k;
  const float s0 = (q & 1) ? cs : sn, c0 = (q & 1) ? sn : cs;
  s = (q & 2) ? -s0 : s0;
  c = ((q + 1) & 2) ? -c0 : c0;
}
DN void chan_consts(u16 ldt, u16 lre, u16 lim, cpx& lam, cpx& coef) {
  const float dt = rbf(exp_acc(bf2f(ldt)));
  const float lr = bf2f(lre), li = bf2f(lim);
  const float e = exp_acc(dt * lr);
  float sn, cs;
  sincos_acc(dt * li, sn, cs);
  lam = {mul_(e, cs), mul_(e, sn)};
  const float nr = lam.re - 1.f, ni = lam.im, den = fma_(lr, lr, mul_(li, li));
  coef = {fma_(nr, lr, mul_(ni, li)) / den, fnma_(nr, li, mul_(ni, lr)) / den};
}
DN f32x2 pk_fma(f32x2 a, f32x2 b, f32x2 c) {
  f32x2 d;
  asm("v_pk_fma_f32 %0, %1, %2, %3" : "=v"(d) : "v"(a), "v"(b), "v"(c));
  return d;
}
DN f32x2 pk_mul(f32x2 a, f32x2 b) {
  f32x2 d;
  asm("v_pk_mul_f32 %0, %1, %2" : "=v"(d) : "v"(a), "v"(b));
  return d;
}
template <int HI, int NEG>
DN f32x2 pk_fma_b(f32x2 k, f32x2 b, f32x2 c) {
  f32x2 d;
  if (HI && NEG) asm("v_pk_fma_f32 %0, %1, %2, %3 op_sel:[1,0,0] neg_lo:[1,0,0] neg_hi:[1,0,0]" : "=v"(d) : "v"(k), "v"(b), "v"(c));
  else if (HI) asm("v_pk_fma_f32 %0, %1, %2, %3 op_sel:[1,0,0]" : "=v"(d) : "v"(k), "v"(b), "v"(c));
  else if (NEG) asm("v_pk_fma_f32 %0, %1, %2, %3 op_sel_hi:[0,1,1] neg_lo:[1,0,0] neg_hi:[1,0,0]" : "=v"(d) : "v"(k), "v"(b), "v"(c));
  else asm("v_pk_fma_f32 %0, %1, %2, %3 op_sel_hi:[0,1,1]" : "=v"(d) : "v"(k), "v"(b), "v"(c));
  return d;
}
template <int HI, int NEG>
DN f32x2 pk_mul_b(f32x2 k, f32x2 b) {
  f32x2 d;
  if (HI && NEG) asm("v_pk_mul_f32 %0, %1, %2 op_sel:[1,0] neg_lo:[1,0] neg_hi:[1,0]" : "=v"(d) : "v"(k), "v"(b));
  else if (HI) asm("v_pk_mul_f32 %0, %1, %2 op_sel:[1,0]" : "=v"(d) : "v"(k), "v"(b));
  else if (NEG) asm("v_pk_mul_f32 %0, %1, %2 op_sel_hi:[0,1] neg_lo:[1,0] neg_hi:[1,0]" : "=v"(d) : "v"(k), "v"(b));
  else asm("v_pk_mul_f32 %0, %1, %2 op_sel_hi:[0,1]" : "=v"(d) : "v"(k), "v"(b));
  return d;
}
DN cpx to_c(f32x2 v) { return {v[0], v[1]}; }
DN unsigned relu_pk(unsigned v) {
  typedef __attribute__((ext_vector_type(2))) short s16x2;
  const s16x2 z = {0, 0};
  return __builtin_bit_cast(unsigned, __builtin_elementwise_max(__builtin_bit_cast(s16x2, v), z));
}
DN f32x2 lo2(f32x4 v) { return __builtin_shufflevector(v, v, 0, 1); }
DN f32x2 hi2(f32x4 v) { return __builtin_shufflevector(v, v, 2, 3); }
DN float sum8(u32x4 v) {
  return ((lo_f(v[0]) + hi_f(v[0])) + (lo_f(v[1]) + hi_f(v[1]))) + ((lo_f(v[2]) + hi_f(v[2])) + (lo_f(v[3]) + hi_f(v[3])));
}
DN float red8(float v, int l) {
  v += lane_get(v, l ^ 1);
  v += lane_get(v, l ^ 2);
  v += lane_get(v, l ^ 4);
  return v;
}
DN char* job_tile(char* sm, int j) { return sm + (j < 8 ? j * JOBSZ : LD_AUX + (j - 8) * JOBSZ); }
DN int kf_start(int w) { return w < 2 ? NF * w : 2 * NF + (NF - 1) * (w - 2); }

template <bool BU>
DN void p1_gemm(const Args& a, char* sm, int ti, int tj, int w, int l) {
  const int wm = w >> 1, wn = w & 1, r = l & 15, g = l >> 4, jb = tj - 9;
  int xo[2], wo[2][2];
#pragma unroll
  for (int kk = 0; kk < 2; ++kk) {
    xo[kk] = (64 * wm + r) * 128 + (((4 * kk + g) ^ (r & 7)) << 4);
    if (BU) {
      wo[kk][0] = 16384 + (96 * wn + r) * 128 + (((4 * kk + g) ^ (r & 7)) << 4);
      wo[kk][1] = 0;
    } else {
      const int q = r >> 2, p = r & 3, f = (q >> 1) | ((g & 1) << 1);
      const int base = 16384 + (4 * kk + g) * 3072 + q * 128 + 8 * p;
      wo[kk][0] = base + (((2 * wn) ^ f) << 5);
      wo[kk][1] = base + (((2 * wn + 1) ^ f) << 5);
    }
  }
  bf16x8 xf[2][4], wf[2][6];
  auto rd = [&](int s, int h, int i) NOPK {
    const char* slot = sm + (s % 3) * SLOT;
    if (i >= 1 && i <= 4) {
      xf[h][i - 1] = *(const bf16x8*)(slot + xo[h] + (i - 1) * 2048);
    } else {
      const int nt = i == 0 ? 0 : i - 4;
      if (BU) wf[h][nt] = *(const bf16x8*)(slot + wo[h][0] + nt * 2048);
      else {
        const char* b = slot + wo[h][nt & 1] + (nt >> 1) * 1024;
        wf[h][nt] = tr_frag(b, b + 512);
      }
    }
  };
  f32x4 acc[6][4];
  const f32x4 z4 = {0.f, 0.f, 0.f, 0.f};
  u32x2 fl = {0u, 0u};
  auto half = [&](int h, bool first, bool pre, int ps, int ph) NOPK {
#pragma unroll
    for (int n = 0; n < 6; ++n)
#pragma unroll
      for (int m = 0; m < 4; ++m) {
        const int idx = 4 * n + m;
        if (pre && idx < 10) rd(ps, ph, idx);
        if (h == 0 && idx == 10) lds_st(sm + SY_FREE + 4 * w, ps + 1);
        if (h == 0 && idx == 16) fl = lds_ld<u32x2>(sm + (ps < 9 ? SY_FULL : SY_GATH));
        if (BU) acc[n][m] = MFMA(xf[h][m], wf[h][n], first ? z4 : acc[n][m]);
        else acc[n][m] = MFMA(wf[h][n], xf[h][m], first ? z4 : acc[n][m]);
        if (idx < 12 || m == 3 || idx == 16) SB();
      }
  };
  bar();
  lds_wait2<1, 0>(lds_addr(sm + SY_FULL), 0u, 0u);
  STAMP(1);
#pragma unroll
  for (int i = 0; i < 10; ++i) rd(0, 0, i);
  SB();
  auto stage = [&](auto sc) NOPK {
    constexpr int s = decltype(sc)::value;
    half(0, s == 0, true, s, 1);
    if (s < 9) lds_wait2<s + 2, 0>(lds_addr(sm + SY_FULL + 4 * ((s + 1) & 1)), fl[(s + 1) & 1], fl[(s + 1) & 1]);
    else lds_wait2<1, 4>(lds_addr(sm + SY_GATH), fl[0], fl[1]);
    SB();
    half(1, false, s < 9, s + 1, 0);
  };
  stage(IC<0>{}); stage(IC<1>{}); stage(IC<2>{}); stage(IC<3>{}); stage(IC<4>{});
  stage(IC<5>{}); stage(IC<6>{}); stage(IC<7>{}); stage(IC<8>{}); stage(IC<9>{});
  SB();
  asm volatile("s_nop 15");
  SB();
  STAMP(2);

  const u64 tag = *(const u64*)(sm + SY_TAG);
  const float* RM = (const float*)(sm + LD_RM);
  const float* RI = (const float*)(sm + LD_RI);
  const float* CS = (const float*)(sm + LD_CS);
  const int rt = 2 * ti + wm;
  const rsrc_t rws = mk_rsrc(a.ws, WS_END);
  if (!BU) {
    f32x2 mu[4];
#pragma unroll
    for (int m = 0; m < 4; ++m) {
      const float t = -RM[64 * wm + 16 * m + r];
      mu[m] = (f32x2){t, t};
    }
#pragma unroll
    for (int pc = 0; pc < 3; ++pc) {
      const f32x4 c0 = *(const f32x4*)(CS + 64 * pc + 32 * wn + 4 * g);
      const f32x4 c1 = *(const f32x4*)(CS + 64 * pc + 32 * wn + 16 + 4 * g);
      const int ks = 6 * tj + 2 * pc + wn;
#pragma unroll
      for (int m = 0; m < 4; ++m) {
        const f32x2 v0 = pk_fma(lo2(c0), mu[m], lo2(acc[2 * pc][m])), v1 = pk_fma(hi2(c0), mu[m], hi2(acc[2 * pc][m]));
        const f32x2 v2 = pk_fma(lo2(c1), mu[m], lo2(acc[2 * pc + 1][m])), v3 = pk_fma(hi2(c1), mu[m], hi2(acc[2 * pc + 1][m]));
        const u32x4 o = {relu_pk(pk_bf16(v0[0], v0[1])), relu_pk(pk_bf16(v1[0], v1[1])), relu_pk(pk_bf16(v2[0], v2[1])),
                         relu_pk(pk_bf16(v3[0], v3[1]))};
        __builtin_amdgcn_raw_buffer_store_b128(o, rws, WS_H1F + ((rt * KSF + ks) * 4 + m) * 1024 + l * 16, 0, SC1);
      }
    }
    lds_st(sm + SY_FREE + 4 * w, 11u);
    STAMP(22);
    WAIT_VM(0);
    if (l == 0) flag_st(a.ws + WS_FAF + (rt * 32 + 2 * tj + wn) * 8, tag);
  } else {
    f32x2 rm[8], ri[8];
#pragma unroll
    for (int m = 0; m < 4; ++m) {
      const f32x4 t0 = *(const f32x4*)(RM + 64 * wm + 16 * g + 4 * m), t1 = *(const f32x4*)(RI + 64 * wm + 16 * g + 4 * m);
      rm[2 * m] = lo2(t0); rm[2 * m + 1] = hi2(t0); ri[2 * m] = lo2(t1); ri[2 * m + 1] = hi2(t1);
    }
    f32x2 lam[3], cs[3], hre[3][8], him[3][8];
    cpx l8[3], l16[3], l32[3];
#pragma unroll
    for (int c = 0; c < 3; ++c) {
      const int ctl = 3 * wn + c;
      const char* lp = sm + LD_LAM + (16 * ctl + r) * 48;
      const f32x4 t0 = *(const f32x4*)lp, t1 = *(const f32x4*)(lp + 16);
      const f32x2 t2 = *(const f32x2*)(lp + 32);
      lam[c] = lo2(t0); l8[c] = to_c(lo2(t1)); l16[c] = to_c(hi2(t1)); l32[c] = to_c(t2);
      cs[c] = (f32x2){CS[32 * ctl + r], CS[32 * ctl + 16 + r]};
    }
    const u32x4 fr = lds_ld<u32x4>(sm + SY_FREE);
#pragma unroll
    for (int k = 0; k < 8; ++k)
#pragma unroll
      for (int c = 0; c < 3; ++c) {
        const f32x4 ar = acc[2 * c][k >> 1], ai = acc[2 * c + 1][k >> 1];
        f32x2 ur = pk_fma((k & 1) ? hi2(ar) : lo2(ar), ri[k], pk_mul_b<0, 1>(cs[c], rm[k]));
        f32x2 ui = pk_fma((k & 1) ? hi2(ai) : lo2(ai), ri[k], pk_mul_b<1, 1>(cs[c], rm[k]));
        if (k > 0) {
          ur = pk_fma_b<0, 0>(lam[c], hre[c][k - 1], pk_fma_b<1, 1>(lam[c], him[c][k - 1], ur));
          ui = pk_fma_b<0, 0>(lam[c], him[c][k - 1], pk_fma_b<1, 0>(lam[c], hre[c][k - 1], ui));
        }
        hre[c][k] = ur;
        him[c][k] = ui;
      }
    const int nct = (jb == 6 && wn == 1) ? 1 : 3;
    SB();
    cpx h[3], ea[3], ex[3], u[3];
#pragma unroll
    for (int c = 0; c < 3; ++c) {
      ea[c] = {hre[c][7][0], him[c][7][0]};
      h[c] = c_fma(l8[c], ea[c], cpx{hre[c][7][1], him[c][7][1]});
      u[c] = {lane_get(h[c].re, l - 16), lane_get(h[c].im, l - 16)};
    }
#pragma unroll
    for (int c = 0; c < 3; ++c) h[c] = c_sel(g >= 1, c_fma(l16[c], u[c], h[c]), h[c]);
    SB();
#pragma unroll
    for (int c = 0; c < 3; ++c) u[c] = {lane_get(h[c].re, l - 32), lane_get(h[c].im, l - 32)};
#pragma unroll
    for (int c = 0; c < 3; ++c) h[c] = c_sel(g >= 2, c_fma(l32[c], u[c], h[c]), h[c]);
    SB();
#pragma unroll
    for (int c = 0; c < 3; ++c) {
      const int ct = 6 * jb + 3 * wn + c;
      const u32x4 ev = {as_u(h[c].re), (unsigned)tag, as_u(h[c].im), (unsigned)(tag >> 32)};
      __builtin_amdgcn_raw_buffer_store_b128(ev, rws, (c < nct && g == 3) ? WS_E64 + ((ct * 32 + rt) * 16 + r) * 16 : WS_END,
                                             0, SC1);
      ex[c] = {lane_get(h[c].re, l - 16), lane_get(h[c].im, l - 16)};
    }
    STAMP(22);
    SB();
    lds_wait2<10, 4>(lds_addr(sm + SY_FREE), fr[0], fr[1]);
    lds_wait2<10, 4>(lds_addr(sm + SY_FREE + 8), fr[2], fr[3]);
#pragma unroll
    for (int c = 0; c < 3; ++c) {
      char* jt = job_tile(sm, 3 * w + c);
#pragma unroll
      for (int q = 0; q < 2; ++q) {
        u32x4 o0, o1;
#pragma unroll
        for (int i = 0; i < 4; ++i) {
          o0[i] = pk_bf16(hre[c][4 * q + i][0], hre[c][4 * q + i][1]);
          o1[i] = pk_bf16(him[c][4 * q + i][0], him[c][4 * q + i][1]);
        }
        *(u32x4*)(jt + q * 1024 + l * 16) = o0;
        *(u32x4*)(jt + (2 + q) * 1024 + l * 16) = o1;
      }
      ex[c] = c_sel(g == 0, cpx{0.f, 0.f}, ex[c]);
      *(f32x4*)(jt + 4096 + l * 16) = (f32x4){ex[c].re, ex[c].im, ea[c].re, ea[c].im};
    }
    lds_st(sm + SY_FREE + 4 * w, 11u);
    STAMP(23);
    cpx l256[3], fac[3], H[3];
    const cpx one = {1.f, 0.f};
    const int ep = (rt - 1 - g) & 3;
#pragma unroll
    for (int c = 0; c < 3; ++c) {
      const cpx l64 = c_mul(l32[c], l32[c]), l128 = c_mul(l64, l64);
      l256[c] = c_mul(l128, l128);
      fac[c] = c_mul(c_sel(ep & 1, l64, one), c_sel(ep & 2, l128, one));
      asm volatile("" : "+v"(l256[c].re), "+v"(l256[c].im), "+v"(fac[c].re), "+v"(fac[c].im));
      H[c] = {0.f, 0.f};
    }
    __builtin_amdgcn_s_sleep(E64_DELAY);
    u32x4 e[3][8];
    for (unsigned spins = 0;; ++spins) {
      unsigned bad = 0u;
#pragma unroll
      for (int i = 0; i < 8; ++i)
#pragma unroll
        for (int c = 0; c < 3; ++c)
          e[c][i] = __builtin_amdgcn_raw_buffer_load_b128(
              rws, (4 * i + g < rt && c < nct) ? WS_E64 + (((6 * jb + 3 * wn + c) * 32 + 4 * i + g) * 16 + r) * 16 : WS_END,
              0, SC1);
#pragma unroll
      for (int i = 0; i < 8; ++i) {
        unsigned b = 0u;
#pragma unroll
        for (int c = 0; c < 3; ++c)
          b |= c < nct ? ((e[c][i][1] ^ (unsigned)tag) | (e[c][i][3] ^ (unsigned)(tag >> 32))) : 0u;
        bad |= 4 * i + g < rt ? b : 0u;
      }
      if (__builtin_amdgcn_ballot_w64(bad != 0u) == 0 || spins >= SPIN_MAX) break;
      __builtin_amdgcn_s_sleep(POLL_SLEEP);
    }
    STAMP(16);
#pragma unroll
    for (int i = 0; i < 8; ++i)
#pragma unroll
      for (int c = 0; c < 3; ++c)
        H[c] = c_sel(4 * i + g < rt, c_fma(l256[c], H[c], cpx{as_f(e[c][i][0]), as_f(e[c][i][2])}), H[c]);
#pragma unroll
    for (int c = 0; c < 3; ++c) H[c] = c_mul(fac[c], H[c]);
#pragma unroll
    for (int c = 0; c < 3; ++c) { H[c].re += lane_get(H[c].re, l ^ 16); H[c].im += lane_get(H[c].im, l ^ 16); }
#pragma unroll
    for (int c = 0; c < 3; ++c) { H[c].re += lane_get(H[c].re, l ^ 32); H[c].im += lane_get(H[c].im, l ^ 32); }
#pragma unroll
    for (int c = 0; c < 3; ++c) *(f32x2*)(sm + LD_CAR + ((3 * w + c) * 16 + r) * 8) = (f32x2){H[c].re, H[c].im};
    lds_st(sm + SY_CAR + 4 * w, 1u);
  }
  STAMP(3);
}

DN void p2_gemm(const Args& a, char* sm, int rt, int cg, int w, int l) {
  const int kf0 = kf_start(w), nf = w < 2 ? NF : NF - 1;
  const rsrc_t raf = mk_rsrc(a.ws + WS_H1F + (rt * KSF + kf0) * 4096, nf * 4096);
  const rsrc_t rbf_ = mk_rsrc(a.ws + WS_W2F + (cg * KSF + kf0) * 5120, nf * 5120);
  const rsrc_t ray = mk_rsrc(a.ws + WS_HF + (rt * KSY + NY * w) * 4096, NY * 4096);
  const rsrc_t rby = mk_rsrc(a.ws + WS_CF + (cg * KSY + NY * w) * 5120, NY * 5120);
  f32x4 acc[5][4];
  bf16x8 fa[PD][4], fb[PD][5];
  const int vo = l * 16;
  char* wreg = sm + LD_P2 + w * WREG;
  auto from_lds = [](int k) NOPK { return k < 12 && k % 3 == 1; };
  auto ld_b = [&](int k, int j) NOPK {
    if (from_lds(k)) {
      fb[k % PD][j] = *(const bf16x8*)(wreg + (k / 3) * 5120 + j * 1024 + vo);
    } else {
      const int so = (k < NF ? k : k - NF) * 5120 + (j >> 2) * 4096;
      fb[k % PD][j] = __builtin_bit_cast(
          bf16x8, __builtin_amdgcn_raw_buffer_load_b128(k < NF ? rbf_ : rby, vo + (j & 3) * 1024, so, SC1));
    }
  };
  auto ld_a = [&](int k) NOPK {
#pragma unroll
    for (int i = 0; i < 4; ++i)
      fa[k % PD][i] = __builtin_bit_cast(
          bf16x8, __builtin_amdgcn_raw_buffer_load_b128(k < NF ? raf : ray, vo + i * 1024, (k < NF ? k : k - NF) * 4096, SC1));
  };
  char* red = wreg + (l & 15) * RED_ROW + (l >> 4) * 16;
  auto dump = [&]() NOPK {
#pragma unroll
    for (int j = 0; j < 5; ++j)
#pragma unroll
      for (int i = 0; i < 4; ++i) *(f32x4*)(red + i * 16 * RED_ROW + j * 64) = acc[j][i];
  };
  {
    const unsigned pt = lds_ld<unsigned>(sm + SY_PTOK);
    lds_wait2<1, 0>(lds_addr(sm + SY_PTOK), pt, pt);
  }
#pragma unroll
  for (int k = 0; k < PD; ++k)
    if (!from_lds(k)) {
#pragma unroll
      for (int j = 0; j < 5; ++j) ld_b(k, j);
    }
  SB();
  {
    const u32x4 go = lds_ld<u32x4>(sm + SY_P2GO);
    lds_wait2<1, 4>(lds_addr(sm + SY_P2GO), go[0], go[1]);
    lds_wait2<1, 0>(lds_addr(sm + SY_P2GO + 8), go[2], go[2]);
  }
  SB();
  STAMP(4);
#pragma unroll
  for (int k = 0; k < PD; ++k) {
    ld_a(k);
    if (from_lds(k)) {
#pragma unroll
      for (int j = 0; j < 5; ++j) ld_b(k, j);
    }
  }
  SB();
  const f32x4 z4 = {0.f, 0.f, 0.f, 0.f};
  unsigned yr = 0u;
#pragma unroll
  for (int k = 0; k < NF + NY; ++k) {
    const bool first = (k == 0 || k == NF);
#pragma unroll
    for (int j = 0; j < 5; ++j) {
#pragma unroll
      for (int i = 0; i < 4; ++i) acc[j][i] = MFMA(fb[k % PD][j], fa[k % PD][i], first ? z4 : acc[j][i]);
      SB();
      if (k + PD < NF + NY) ld_b(k + PD, j);
      SB();
    }
    if (k == YPOLL_AT) lds_st(sm + SY_YRDY + 4, 1u);
    if (k + PD == NF - 1) yr = lds_ld<unsigned>(sm + SY_YRDY);
    if (k + PD == NF) {
      STAMP(5);
      lds_wait2<1, 0>(lds_addr(sm + SY_YRDY), yr, yr);
      STAMP(6);
    }
    if (k + PD < NF + NY) ld_a(k + PD);
    SB();
    if (k == NF - 1) {
      dump();
      bar();
      STAMP(7);
    }
  }
  STAMP(8);
  bar();
  dump();
  bar();
  STAMP(9);
}

struct Early { u32x2 xv[5], dv[5]; };
DN void early_loads(const Args& a, int rt, int cg, int hw, int l, Early& y) {
#pragma unroll
  for (int e = 0; e < 5; ++e) {
    const int idx = hw * 64 + l + 256 * e, row = idx / 20, c4 = idx - row * 20;
    y.xv[e] = *(const u32x2*)(a.x + (size_t)(rt * 64 + row) * D_ + cg * 80 + c4 * 4);
    y.dv[e] = *(const u32x2*)(a.dsk + cg * 80 + c4 * 4);
  }
}
template <bool BU>
DN void p1_loader(const Args& a, char* sm, int ti, int tj, int rt, int cg, int lw, int l, Early& y) {
  const int l3 = l >> 3, l7 = l & 7, jb = tj - 9;
  const rsrc_t rx = mk_rsrc(a.x + (size_t)ti * 128 * D_, 128 * D_ * 2);
  const int vx = (BU ? (16 * (l3 >> 2) + 8 * (l3 & 1) + ((l3 >> 1) & 1)) : l3) * (D_ * 2) + ((l7 ^ l3) << 4);
  rsrc_t rw0, rw1;
  int vw0, vw1 = 0, qmax = 5;
  if (BU) {
    rw0 = mk_rsrc(a.bre, P_ * D_ * 2);
    rw1 = mk_rsrc(a.bim, P_ * D_ * 2);
    vw0 = (96 * jb + l3) * (D_ * 2) + ((l7 ^ l3) << 4);
    qmax = (jb == 6) ? 3 : 5;
  } else {
    rw0 = mk_rsrc(a.w1, D_ * F_ * 2);
    rw1 = rw0;
    const int f0 = (l3 >> 1) & 1;
    const int base = l3 * (F_ * 2) + tj * 384 + (l7 & 1) * 16;
    vw0 = base + (((l7 >> 1) ^ f0) << 5);
    vw1 = base + (((l7 >> 1) ^ (f0 | 2)) << 5);
  }
  auto stage = [&](int s, int slot_i) NOPK {
    char* slot = sm + slot_i * SLOT;
#pragma unroll
    for (int v = 0; v < 4; ++v) {
#pragma unroll
      for (int q = 0; q < 4; ++q) {
        const int rowoff = BU ? (64 * (q >> 1) + 4 * (q & 1) + 32 * (v & 1) + 2 * (v >> 1)) : (32 * q + 8 * v);
        glds16<0>(rx, slot + (4 * q + v) * 1024, vx, rowoff * (D_ * 2) + s * 128);
      }
#pragma unroll
      for (int q = 0; q < 6; ++q) {
        if (BU) {
          const int qe = q < qmax ? q : qmax;
          glds16<0>(v < 2 ? rw0 : rw1, slot + 16384 + (4 * q + v) * 1024, vw0, (qe * 16 + 8 * (v & 1)) * (D_ * 2) + s * 128);
        } else {
          glds16<0>(rw0, slot + 16384 + (6 * v + q) * 1024, q < 3 ? vw0 : vw1,
                    (s * 64 + 16 * v + 8 * (q / 3)) * (F_ * 2) + (q % 3) * 128);
        }
      }
    }
  };
  const rsrc_t rb = mk_rsrc(a.ws + WS_W2F + cg * KSF * 5120, KSF * 5120);
  auto prefetch = [&](int g0, int n) NOPK {
#pragma unroll 1
    for (int gi = g0 + lw; gi < g0 + n; gi += 2) {
      const int gw = gi >> 2, gq = gi & 3;
      const int so = (kf_start(gw) + 3 * gq + 1) * 5120;
#pragma unroll
      for (int j = 0; j < 5; ++j) glds16<SC1>(rb, sm + LD_P2 + gw * WREG + gq * 5120 + j * 1024, l * 16, so + j * 1024);
    }
  };
  bar();
  __builtin_amdgcn_s_setprio(3);
  int slot_i = lw;
#pragma unroll 1
  for (int t = lw; t < 10; t += 2) {
    if (t >= 3) lds_wait(sm + SY_FREE, t - 2, lds_ld<u32x4>(sm + SY_FREE));
    stage(t, slot_i);
    WAIT_VM(0);
    lds_st(sm + SY_FULL + 4 * lw, t + 1);
    slot_i = slot_i == 0 ? 2 : slot_i - 1;
  }
  lds_wait(sm + SY_PTOK, 1u, lds_ld<unsigned>(sm + SY_PTOK));
  lds_wait(sm + SY_FREE, 8u, lds_ld<u32x4>(sm + SY_FREE));
  prefetch(0, 7);
  lds_wait(sm + SY_FREE, 9u, lds_ld<u32x4>(sm + SY_FREE));
  prefetch(7, 9);
  early_loads(a, rt, cg, lw, l, y);
  WAIT_VM(0);
  lds_st(sm + SY_P2GO + 4 * lw, 1u);
}

template <bool BU>
DN void p1_side(const Args& a, char* sm, int ti, int tj, int rt, int cg, int sw, int l) {
  const int jb = tj - 9, r = l & 15, g = l >> 4, l3 = l >> 3, l7 = l & 7;
  const int bl = ti * 16 + tj, pcg = bl & 7, pq = bl >> 3;
  const rsrc_t rws = mk_rsrc(a.ws, WS_END);
  if (sw == 0 && l < 32) lds_st(sm + LD_SYNC + 4 * l, l == 19 ? 1u : 0u);
  bar();
  SB();
  u32x4 va[20];
  {
    const char *p0, *p1;
    int step;
    if (sw == 0) {
      p0 = (const char*)(a.x + (size_t)(ti * 128 + 8 * tj + l3) * D_) + l7 * 16;
      p1 = p0;
      step = 128;
    } else if (BU) {
      const char* pp[2];
#pragma unroll
      for (int h = 0; h < 2; ++h) {
        int n = 12 * ti + 8 * h + l3;
        n = (8 * h + l3 < 12) ? n : 12 * ti;
        int ch = 96 * jb + 16 * (n >> 5) + (n & 15);
        ch = ch < P_ ? ch : P_ - 1;
        pp[h] = (const char*)(((n >> 4) & 1 ? a.bim : a.bre) + (size_t)ch * D_) + l7 * 16;
      }
      p0 = pp[0]; p1 = pp[1];
      step = 128;
    } else {
      const int lc = l < 48 ? l : l - 48, c = lc % 24, rg = lc / 24;
      p0 = (const char*)(a.w1 + (size_t)(40 * ti + 20 * rg) * F_ + 192 * tj) + c * 16;
      step = F_ * 2;
      p1 = p0 + 10 * step;
    }
#pragma unroll
    for (int i = 0; i < 10; ++i) va[i] = *(const u32x4*)(p0 + i * step);
    if (sw) {
#pragma unroll
      for (int i = 0; i < 10; ++i) va[10 + i] = *(const u32x4*)(p1 + i * step);
    }
  }
  const u64 ep = flag_ld(a.ws + WS_EP);
  u16 c_ldt = 0, c_lre = 0, c_lim = 0;
  if (BU) {
    int p = 96 * jb + 48 * sw + (l < 48 ? l : 0);
    p = p < P_ ? p : P_ - 1;
    c_ldt = a.ldt[p]; c_lre = a.lre[p]; c_lim = a.lim[p];
  }
  SB();
  u32x4 vc[10];
  const int cks0 = 3 * (pq - 18), cfr = pq < 31 ? 15 : 5;
  auto piece = [&](int e) NOPK {
    const int pc = sw ? 10 + (e % 5) : e;
    return pc < cfr ? pc : cfr - 1;
  };
  if (pq < 18) {
    const rsrc_t r2 = mk_rsrc(a.w2, F_ * D_ * 2);
#pragma unroll
    for (int e = 0; e < 10; ++e) {
      const int pc = piece(e);
      const int q = pc * 64 + l, row = q / 10, ch = q - row * 10;
      if (e < 5 || sw == 0)
        glds16<0>(r2, sm + LD_AUX + pc * 1024, (96 * pq + row) * (D_ * 2) + pcg * 160 + ch * 16, 0);
    }
  } else {
    const u16* cm = g < 2 ? a.cre : a.cim;
#pragma unroll
    for (int e = 0; e < 10; ++e) {
      const int pc = piece(e), ksl = pc / 5, nt = pc - 5 * ksl;
      glds16_g(cm + (size_t)(80 * pcg + 16 * nt + r) * P_ + 16 * (cks0 + ksl) + 8 * (g & 1), sm + LD_AUX + pc * 1024,
               e < 5 || sw == 0);
    }
  }
  SB();
  WAIT_VM(0);
  if (sw == 0) {
    float s = 0.f;
#pragma unroll
    for (int i = 0; i < 10; ++i) s += sum8(va[i]);
    const float mean = red8(s, l) * (1.f / D_);
    float q = 0.f;
#pragma unroll
    for (int i = 0; i < 10; ++i)
#pragma unroll
      for (int e = 0; e < 4; ++e) {
        const float d0 = lo_f(va[i][e]) - mean, d1 = hi_f(va[i][e]) - mean;
        q = __builtin_fmaf(d0, d0, __builtin_fmaf(d1, d1, q));
      }
    const float var = red8(q, l) * (1.f / D_);
    const float sd = rbf(__builtin_sqrtf(rbf(rbf(var) + 1e-5f)));
    const u32x2 o = {as_u(rbf(mean)), as_u(sd)};
    if (l7 == 0) __builtin_amdgcn_raw_buffer_store_b64(o, rws, WS_STAT + (ti * 128 + 8 * tj + l3) * 8, 0, SC1);
    if (l == 0) *(u64*)(sm + SY_TAG) = ep + 0x9E3779B97F4A7C15ull;
    lds_st(sm + SY_TAG + 8, 1u);
  } else if (BU) {
#pragma unroll
    for (int h = 0; h < 2; ++h) {
      float s = 0.f;
#pragma unroll
      for (int i = 0; i < 10; ++i) s += sum8(va[10 * h + i]);
      s = red8(s, l);
      if (l7 == 0 && 8 * h + l3 < 12)
        __builtin_amdgcn_raw_buffer_store_b32(as_u(s), rws, WS_CSS + (jb * 192 + 12 * ti + 8 * h + l3) * 4, 0, SC1);
    }
  } else {
    float s[8];
#pragma unroll
    for (int e = 0; e < 8; ++e) s[e] = 0.f;
#pragma unroll
    for (int i = 0; i < 20; ++i)
#pragma unroll
      for (int e = 0; e < 4; ++e) { s[2 * e] += lo_f(va[i][e]); s[2 * e + 1] += hi_f(va[i][e]); }
#pragma unroll
    for (int e = 0; e < 8; ++e) s[e] += lane_get(s[e], l + 24);
    if (l < 24) {
#pragma unroll
      for (int h = 0; h < 2; ++h) {
        const u32x4 o = {as_u(s[4 * h]), as_u(s[4 * h + 1]), as_u(s[4 * h + 2]), as_u(s[4 * h + 3])};
        __builtin_amdgcn_raw_buffer_store_b128(o, rws, WS_CSF + ((tj * 16 + ti) * 192 + l * 8 + 4 * h) * 4, 0, SC1);
      }
    }
  }
  STAMP(12);
  if (pq < 18) {
#pragma unroll
    for (int e = 0; e < 10; ++e) {
      const int pc = piece(e), ksl = pc / 5, nt = pc - 5 * ksl;
      const char* b = sm + LD_AUX + (32 * ksl + 4 * g + (r >> 2)) * 160 + nt * 32 + (r & 3) * 8;
      vc[e] = __builtin_bit_cast(u32x4, tr_frag(b, b + 16 * 160));
    }
  } else {
#pragma unroll
    for (int e = 0; e < 10; ++e) vc[e] = *(const u32x4*)(sm + LD_AUX + piece(e) * 1024 + l * 16);
  }
  {
    const unsigned dst = pq < 18 ? WS_W2F + (pcg * KSF + 3 * pq) * 5120 : WS_CF + (pcg * KSY + cks0) * 5120;
#pragma unroll
    for (int e = 0; e < 10; ++e)
      if (e < 5 || sw == 0)
        __builtin_amdgcn_raw_buffer_store_b128(vc[e], rws, dst + piece(e) * 1024 + l * 16, 0, SC1);
  }
  if (BU) {
    cpx lam, coef;
    chan_consts(c_ldt, c_lre, c_lim, lam, coef);
    const cpx l2 = c_mul(lam, lam), l4 = c_mul(l2, l2), l8 = c_mul(l4, l4), l16 = c_mul(l8, l8), l32 = c_mul(l16, l16);
    if (l < 48) {
      char* lp = sm + LD_LAM + (48 * sw + l) * 48;
      *(f32x4*)lp = (f32x4){lam.re, lam.im, coef.re + coef.re, coef.im + coef.im};
      *(f32x4*)(lp + 16) = (f32x4){l8.re, l8.im, l16.re, l16.im};
      *(f32x2*)(lp + 32) = (f32x2){l32.re, l32.im};
    }
  }
  lds_wait(sm + SY_TAG + 8, 1u, lds_ld<unsigned>(sm + SY_TAG + 8));
  const u64 tag = *(const u64*)(sm + SY_TAG);
  WAIT_VM(0);
  if (l < 2)
    flag_st(a.ws + (l == 1 ? WS_FPT + ((pcg * 32 + pq) * 2 + sw) * 8
                           : (sw == 0 ? WS_FST + (ti * 16 + tj) * 8 : WS_FCS + (tj * 16 + ti) * 8)), tag);
  STAMP(13);
  const char* fp = a.ws + (sw == 0 ? WS_FST + (l < 16 ? ti : (rt >> 1)) * 128 : WS_FCS + tj * 128) + r * 8;
  lds_wait(sm + SY_FREE, GATHER_AT, lds_ld<unsigned>(sm + SY_FREE));
  if (sw == 0) {
    flag_wait(a.ws + WS_FPT + cg * 512 + l * 8, true, tag);
    lds_st(sm + SY_PTOK, 1u);
  }
  STAMP(15);
  flag_wait(fp, l < (sw == 0 ? 32 : 16), tag);
  u32x4 gv[12];
  if (sw == 0) {
    gv[0] = __builtin_amdgcn_raw_buffer_load_b128(rws, WS_STAT + ti * 1024 + l * 16, 0, SC1);
    const u32x2 s2 = __builtin_amdgcn_raw_buffer_load_b64(rws, WS_STAT + rt * 512 + l * 8, 0, SC1);
    *(u32x2*)(sm + LD_ST2 + l * 8) = s2;
#pragma unroll
    for (int h = 0; h < 2; ++h) {
      const float mb = as_f(gv[0][2 * h]), sd = as_f(gv[0][2 * h + 1]);
      const float ri = 1.f / sd;
      const int t = 2 * l + h;
      const int pos = BU ? ((t & ~15) + ((t & 6) << 1) + ((t & 1) << 1) + ((t >> 3) & 1)) : t;
      ((float*)(sm + LD_RM))[pos] = BU ? mb * ri : mb;
      ((float*)(sm + LD_RI))[pos] = ri;
    }
  } else if (BU) {
    gv[0] = __builtin_amdgcn_raw_buffer_load_b128(rws, WS_CSS + jb * 768 + (l < 48 ? l : 0) * 16, 0, SC1);
    if (l < 48) *(u32x4*)(sm + LD_CS + l * 16) = gv[0];
  } else {
#pragma unroll
    for (int i = 0; i < 12; ++i)
      gv[i] = __builtin_amdgcn_raw_buffer_load_b128(
          rws, WS_CSF + (tj * 16 + 4 * (i / 3) + g) * 768 + (r + 16 * (i % 3)) * 16, 0, SC1);
#pragma unroll
    for (int c = 0; c < 3; ++c) {
      f32x4 s;
#pragma unroll
      for (int e = 0; e < 4; ++e) {
        float t = (as_f(gv[c][e]) + as_f(gv[3 + c][e])) + (as_f(gv[6 + c][e]) + as_f(gv[9 + c][e]));
        t += lane_get(t, l ^ 16);
        t += lane_get(t, l ^ 32);
        s[e] = t;
      }
      if (g == 0) *(f32x4*)(sm + LD_CS + (r + 16 * c) * 16) = s;
    }
  }
  asm volatile("" ::: "memory");
  lds_st(sm + SY_GATH + 4 * sw, 1u);
  STAMP(14);
}

DN void faf_poll(const Args& a, char* sm, int rt, int l, u64 tag) {
  __builtin_amdgcn_s_sleep(FAF_DELAY);
  flag_wait(a.ws + WS_FAF + (rt * 32 + (l & 31)) * 8, l < 18, tag);
  lds_st(sm + SY_P2GO + 8, 1u);
}
DN void p2_helper(const Args& a, char* sm, int ti, int tj, int rt, int cg, int hw, int l, const Early& y) {
  lds_wait(sm + SY_GATH, 1u, lds_ld<u32x2>(sm + SY_GATH));
  const u64 tag = *(const u64*)(sm + SY_TAG);
  const rsrc_t rws = mk_rsrc(a.ws, WS_END);
  const int ht = hw * 64 + l;
  STAMP(3);
  if (tj >= 9) {
    const int jb = tj - 9, wn = hw & 1, rj = 2 * ti + (hw >> 1), ct0 = 6 * jb + 3 * wn, c16 = l & 15, g = l >> 4;
    const int nct = (jb == 6 && wn == 1) ? 1 : 3;
    cpx lg[3], l8[3];
    f32x2 cf[3], wk[3][8];
    const cpx one = {1.f, 0.f};
#pragma unroll
    for (int c = 0; c < 3; ++c) {
      const char* lp = sm + LD_LAM + (16 * (3 * wn + c) + c16) * 48;
      const f32x4 t0 = *(const f32x4*)lp, t1 = *(const f32x4*)(lp + 16);
      const cpx l32 = to_c(*(const f32x2*)(lp + 32));
      cf[c] = hi2(t0);
      l8[c] = to_c(lo2(t1));
      lg[c] = c_mul(c_sel(g & 1, to_c(hi2(t1)), one), c_sel(g & 2, l32, one));
      cpx t = to_c(cf[c]);
#pragma unroll
      for (int k = 0; k < 8; ++k) {
        t = c_mul(to_c(lo2(t0)), t);
        wk[c][k] = (f32x2){t.re, t.im};
        asm volatile("" : "+v"(wk[c][k]));
      }
      asm volatile("" : "+v"(lg[c].re), "+v"(lg[c].im));
    }
    lds_wait(sm + SY_FREE + 4 * hw, 11u, lds_ld<unsigned>(sm + SY_FREE + 4 * hw));
    __builtin_amdgcn_s_setprio(3);
    STAMP(4);
    f32x2 bre[3][8], bim[3][8];
    f32x4 pe[3];
#pragma unroll
    for (int c = 0; c < 3; ++c) {
      const char* jt = job_tile(sm, 3 * hw + c);
      u32x4 h[4];
#pragma unroll
      for (int q = 0; q < 4; ++q) h[q] = *(const u32x4*)(jt + q * 1024 + l * 16);
      pe[c] = *(const f32x4*)(jt + 4096 + l * 16);
#pragma unroll
      for (int k = 0; k < 8; ++k) {
        const unsigned ur = h[k >> 2][k & 3], ui = h[2 + (k >> 2)][k & 3];
        const f32x2 hr = {lo_f(ur), hi_f(ur)}, hi = {lo_f(ui), hi_f(ui)};
        bre[c][k] = pk_fma_b<1, 1>(cf[c], hi, pk_mul_b<0, 0>(cf[c], hr));
        bim[c][k] = pk_fma_b<1, 1>(cf[c], hr, pk_mul_b<0, 1>(cf[c], hi));
        asm volatile("" : "+v"(bre[c][k]), "+v"(bim[c][k]));
      }
    }
    SB();
    STAMP(24);
    if (hw == 2) faf_poll(a, sm, rt, l, tag);
    lds_wait(sm + SY_CAR + 4 * hw, 1u, lds_ld<unsigned>(sm + SY_CAR + 4 * hw));
    cpx H[3];
#pragma unroll
    for (int c = 0; c < 3; ++c) H[c] = to_c(*(const f32x2*)(sm + LD_CAR + ((3 * hw + c) * 16 + c16) * 8));
    STAMP(17);
#pragma unroll
    for (int c = 0; c < 3; ++c) {
      const cpx ca = c_fma(lg[c], H[c], to_c(lo2(pe[c]))), cb = c_fma(l8[c], ca, to_c(hi2(pe[c])));
      const f32x2 cre = {ca.re, cb.re}, cim = {ca.im, cb.im};
#pragma unroll
      for (int k = 0; k < 8; ++k) {
        bre[c][k] = pk_fma_b<1, 1>(wk[c][k], cim, pk_fma_b<0, 0>(wk[c][k], cre, bre[c][k]));
        bim[c][k] = pk_fma_b<1, 1>(wk[c][k], cre, pk_fma_b<0, 1>(wk[c][k], cim, bim[c][k]));
      }
      char* jt = job_tile(sm, 3 * hw + c);
#pragma unroll
      for (int q = 0; q < 2; ++q) {
        u32x4 o0, o1;
#pragma unroll
        for (int i = 0; i < 4; ++i) {
          o0[i] = pk_bf16(bre[c][2 * i][q], bre[c][2 * i + 1][q]);
          o1[i] = pk_bf16(bim[c][2 * i][q], bim[c][2 * i + 1][q]);
        }
        *(u32x4*)(jt + c16 * 144 + g * 32 + q * 16) = o0;
        *(u32x4*)(jt + (16 + c16) * 144 + g * 32 + q * 16) = o1;
      }
    }
    SB();
    u32x4 v[3][4];
#pragma unroll
    for (int c = 0; c < 3; ++c) {
      const char* b = job_tile(sm, 3 * hw + c) + (8 * g + (c16 >> 2)) * 144 + (c16 & 3) * 8;
#pragma unroll
      for (int mt = 0; mt < 4; ++mt) v[c][mt] = __builtin_bit_cast(u32x4, tr_frag(b + mt * 32, b + mt * 32 + 4 * 144));
    }
    SB();
#pragma unroll
    for (int c = 0; c < 3; ++c)
#pragma unroll
      for (int mt = 0; mt < 4; ++mt)
        __builtin_amdgcn_raw_buffer_store_b128(
            v[c][mt], rws, (c < nct ? WS_HF + ((rj * KSY + ct0 + c) * 4 + mt) * 1024 : WS_END) + l * 16, 0, SC1);
    __builtin_amdgcn_s_setprio(0);
    STAMP(25);
    WAIT_VM(0);
    if (l == 0) flag_st(a.ws + WS_FH + (rj * 32 + 2 * jb + wn) * 8, tag);
  } else if (hw == 2) {
    lds_wait(sm + SY_FREE + 4 * hw, 11u, lds_ld<unsigned>(sm + SY_FREE + 4 * hw));
    faf_poll(a, sm, rt, l, tag);
  }
  STAMP(5);
  float xs[5][4], xd[5][4], gt[5][4], ri[5];
#pragma unroll
  for (int e = 0; e < 5; ++e) {
    const int row = (ht + 256 * e) / 20;
    const f32x2 st = *(const f32x2*)(sm + LD_ST2 + row * 8);
    xs[e][0] = lo_f(y.xv[e][0]); xs[e][1] = hi_f(y.xv[e][0]); xs[e][2] = lo_f(y.xv[e][1]); xs[e][3] = hi_f(y.xv[e][1]);
    const float ds[4] = {lo_f(y.dv[e][0]), hi_f(y.dv[e][0]), lo_f(y.dv[e][1]), hi_f(y.dv[e][1])};
    const float mb = st[0];
    ri[e] = 1.f / st[1];
#pragma unroll
    for (int i = 0; i < 4; ++i) xd[e][i] = rbf(rbf(rbf(xs[e][i] - mb) * ri[e]) * ds[i]);
    asm volatile("" : "+v"(xd[e][0]), "+v"(xd[e][1]), "+v"(xd[e][2]), "+v"(xd[e][3]), "+v"(ri[e]));
  }
  if (hw == 0) {
    lds_wait(sm + SY_YRDY + 4, 1u, lds_ld<unsigned>(sm + SY_YRDY + 4));
    const char* fp = a.ws + WS_FH + (rt * 32 + (l & 31)) * 8;
    flag_wait(fp, l < 14, tag);
    lds_st(sm + SY_YRDY, 1u);
  }
  if (blockIdx.x == 0 && hw == 1) {
#pragma unroll
    for (int i = 0; i < 4; ++i) {
      const char* fp = a.ws + WS_FST + (i * 64 + l) * 8;
      flag_wait(fp, true, tag);
    }
    if (l == 0) flag_st(a.ws + WS_EP, tag);
  }
  STAMP(6);
  bar();
  STAMP(7);
#pragma unroll
  for (int e = 0; e < 5; ++e) {
    const int idx = ht + 256 * e, row = idx / 20, c4 = idx - row * 20;
    const char* p = sm + LD_P2 + row * RED_ROW + c4 * 16;
    const f32x4 p0 = *(const f32x4*)p, p1 = *(const f32x4*)(p + WREG), p2 = *(const f32x4*)(p + 2 * WREG),
                p3 = *(const f32x4*)(p + 3 * WREG);
#pragma unroll
    for (int i = 0; i < 4; ++i) {
      const float fv = rbf(((p0[i] + p1[i]) + (p2[i] + p3[i])) * ri[e]);
      gt[e][i] = rbf(fv / (1.f + __builtin_amdgcn_exp2f(-1.44269504088896f * fv)));
    }
    asm volatile("" : "+v"(gt[e][0]), "+v"(gt[e][1]), "+v"(gt[e][2]), "+v"(gt[e][3]));
  }
  STAMP(8);
  bar();
  bar();
  STAMP(9);
#pragma unroll
  for (int e = 0; e < 5; ++e) {
    const int idx = ht + 256 * e, row = idx / 20, c4 = idx - row * 20;
    const char* p = sm + LD_P2 + row * RED_ROW + c4 * 16;
    const f32x4 p0 = *(const f32x4*)p, p1 = *(const f32x4*)(p + WREG), p2 = *(const f32x4*)(p + 2 * WREG),
                p3 = *(const f32x4*)(p + 3 * WREG);
    f32x4 o4;
#pragma unroll
    for (int i = 0; i < 4; ++i) o4[i] = xs[e][i] + (((p0[i] + p1[i]) + (p2[i] + p3[i])) + xd[e][i]) * gt[e][i];
    *(f32x4*)(a.out + (size_t)(rt * 64 + row) * D_ + cg * 80 + c4 * 4) = o4;
  }
}

__global__ __launch_bounds__(512, 1) NOPK void fused_kernel(Args a) {
  extern __shared__ __attribute__((aligned(16))) char sm[];
  const int tid = threadIdx.x, l = tid & 63;
  const int w = __builtin_amdgcn_readfirstlane(tid >> 6);
  const int b = blockIdx.x, x = b & 7, o = b >> 3;
  const int ti = (x & 1) * 8 + (o & 7);
  const int tj = (x >> 1) * 4 + (o >> 3);
  const int rt = (x >> 1) * 8 + (o >> 2);
  const int cg = (x & 1) * 4 + (o & 3);
  STAMP(0);
  if (w < 4) {
    __builtin_amdgcn_s_setprio(2);
    if (tj < 9) p1_gemm<false>(a, sm, ti, tj, w, l);
    else p1_gemm<true>(a, sm, ti, tj, w, l);
    p2_gemm(a, sm, rt, cg, w, l);
  } else {
    Early y;
    if (w < 6) {
      if (tj < 9) p1_loader<false>(a, sm, ti, tj, rt, cg, w - 4, l, y);
      else p1_loader<true>(a, sm, ti, tj, rt, cg, w - 4, l, y);
    } else {
      if (tj < 9) p1_side<false>(a, sm, ti, tj, rt, cg, w - 6, l);
      else p1_side<true>(a, sm, ti, tj, rt, cg, w - 6, l);
      early_loads(a, rt, cg, w - 4, l, y);
    }
    STAMP(2);
    __builtin_amdgcn_s_setprio(0);
    p2_helper(a, sm, ti, tj, rt, cg, w - 4, l, y);
  }
  STAMP(10);
  STAMP(11);
}

extern "C" void kernel_launch(void* const* d_in, const int* in_sizes, int n_in, void* d_out, int out_size,
                              void* d_ws, size_t ws_size, hipStream_t stream) {
  (void)in_sizes; (void)n_in; (void)out_size; (void)ws_size;
  Args a;
  a.x = (const u16*)d_in[0]; a.lre = (const u16*)d_in[1]; a.lim = (const u16*)d_in[2];
  a.bre = (const u16*)d_in[3]; a.bim = (const u16*)d_in[4]; a.cre = (const u16*)d_in[5];
  a.cim = (const u16*)d_in[6]; a.ldt = (const u16*)d_in[7]; a.dsk = (const u16*)d_in[8];
  a.w1 = (const u16*)d_in[10]; a.w2 = (const u16*)d_in[11];
  a.ws = (char*)d_ws; a.out = (float*)d_out;
  (void)hipFuncSetAttribute((const void*)fused_kernel, hipFuncAttributeMaxDynamicSharedMemorySize, LDS_BYTES + 512);
  fused_kernel<<<256, 512, LDS_BYTES + 512, stream>>>(a);
}
```

```cpp
#include <hip/hip_runtime.h>
#include <stdint.h>

typedef unsigned short u16;
typedef unsigned long long u64;
typedef __attribute__((ext_vector_type(8))) short bf16x8;
typedef __attribute__((ext_vector_type(4))) short s16x4;
typedef __attribute__((ext_vector_type(4))) float f32x4;
typedef __attribute__((ext_vector_type(2))) float f32x2;
typedef __attribute__((ext_vector_type(4))) unsigned u32x4;
typedef __attribute__((ext_vector_type(2))) unsigned u32x2;
typedef __attribute__((ext_vector_type(2))) __bf16 bf16x2;
typedef __amdgpu_buffer_rsrc_t rsrc_t;
#define LDS_AS __attribute__((address_space(3)))
#define DN __device__ __forceinline__
#define MFMA(a, b, c) __builtin_amdgcn_mfma_f32_16x16x32_bf16((a), (b), (c), 0, 0, 0)
#define SB() __builtin_amdgcn_sched_barrier(0)
#define WAIT_VM(n) asm volatile("s_waitcnt vmcnt(" #n ")" ::: "memory")
#define SC1 16
template <int V> struct IC { static constexpr int value = V; };

constexpr int L_ = 2048, D_ = 640, P_ = 640, F_ = 1728;
constexpr int KSF = F_ / 32;
constexpr int KSY = P_ / 16;

constexpr unsigned WS_H1F = 0;
constexpr unsigned WS_HF = WS_H1F + 32u * KSF * 4096;
constexpr unsigned WS_W2F = WS_HF + 32u * KSY * 4096;
constexpr unsigned WS_CF = WS_W2F + 8u * KSF * 5120;
constexpr unsigned WS_E64 = WS_CF + 8u * KSY * 5120;
constexpr unsigned WS_STAT = WS_E64 + KSY * 32u * 256;
constexpr unsigned WS_CSF = WS_STAT + L_ * 8u;
constexpr unsigned WS_CSS = WS_CSF + 9u * 16 * 768;
constexpr unsigned WS_FST = WS_CSS + 7u * 768 + 768;
constexpr unsigned WS_FCS = WS_FST + 2048;
constexpr unsigned WS_FPT = WS_FCS + 2048;
constexpr unsigned WS_FAF = WS_FPT + 4096;
constexpr unsigned WS_FPC = WS_FAF + 8192;
constexpr unsigned WS_FH = WS_FPC + 4096;
constexpr unsigned WS_EP = WS_FH + 8192;
constexpr unsigned WS_END = WS_EP + 128;

constexpr int SLOT = 40960;
constexpr int WREG = 21504;
constexpr int LD_P2 = SLOT;
constexpr int LD_AUX = LD_P2 + 4 * WREG;
constexpr int JOBSZ = 5120;
constexpr int LD_RM = LD_AUX + 20480;
constexpr int LD_RI = LD_RM + 512;
constexpr int LD_CS = LD_RI + 512;
constexpr int LD_LAM = LD_CS + 768;
constexpr int LD_CAR = LD_LAM + 4608;
constexpr int LD_ST2 = LD_CAR + 1536;
constexpr int LD_SYNC = LD_ST2 + 512;
constexpr int LD_SCR = LD_SYNC + 128;
constexpr int LDS_BYTES = LD_SCR + 256;
constexpr int SY_TAG = LD_SYNC, SY_FULL = LD_SYNC + 16, SY_FREE = LD_SYNC + 32, SY_GATH = LD_SYNC + 48,
              SY_PTOK = LD_SYNC + 56, SY_P2GO = LD_SYNC + 64, SY_YRDY = LD_SYNC + 80, SY_CAR = LD_SYNC + 96;
constexpr int RED_ROW = 336;
constexpr int PD = 3, NF = 14, NY = 10;
constexpr unsigned GATHER_AT = 6u, GATHER1_AT = 5u;
constexpr int YPOLL_AT = 6;
constexpr int PDA = 4;
constexpr int RD_AT = 8;
constexpr unsigned CODEPF = 63488;
constexpr unsigned SPIN_MAX = 1u << 18;

struct Args {
  const u16 *x, *lre, *lim, *bre, *bim, *cre, *cim, *ldt, *dsk, *w1, *w2;
  char* ws;
  float* out;
};

struct cpx { float re, im; };
DN float fma_(float a, float b, float c) {
  float d;
  asm("v_fma_f32 %0, %1, %2, %3" : "=v"(d) : "v"(a), "v"(b), "v"(c));
  return d;
}
DN float fnma_(float a, float b, float c) {
  float d;
  asm("v_fma_f32 %0, -%1, %2, %3" : "=v"(d) : "v"(a), "v"(b), "v"(c));
  return d;
}
DN float mul_(float a, float b) {
  float d;
  asm("v_mul_f32 %0, %1, %2" : "=v"(d) : "v"(a), "v"(b));
  return d;
}
DN cpx c_mul(cpx a, cpx b) { return {fnma_(a.im, b.im, mul_(a.re, b.re)), fma_(a.im, b.re, mul_(a.re, b.im))}; }
DN cpx c_fma(cpx a, cpx b, cpx c) {
  return {fma_(a.re, b.re, fnma_(a.im, b.im, c.re)), fma_(a.re, b.im, fma_(a.im, b.re, c.im))};
}
DN cpx c_sel(bool p, cpx a, cpx b) { return {p ? a.re : b.re, p ? a.im : b.im}; }
DN float lane_get(float v, int src) {
  return __builtin_bit_cast(float, __builtin_amdgcn_ds_bpermute(src << 2, __builtin_bit_cast(int, v)));
}
DN unsigned pk_bf16(float lo, float hi) {
  f32x2 f = {lo, hi};
  return __builtin_bit_cast(unsigned, __builtin_convertvector(f, bf16x2));
}
DN float as_f(unsigned u) { return __builtin_bit_cast(float, u); }
DN unsigned as_u(float f) { return __builtin_bit_cast(unsigned, f); }
DN float lo_f(unsigned u) { return __builtin_bit_cast(float, u << 16); }
DN float hi_f(unsigned u) { return __builtin_bit_cast(float, u & 0xffff0000u); }
DN float bf2f(u16 v) { return lo_f(v); }
DN float rbf(float f) { return lo_f(pk_bf16(f, 0.f)); }
DN rsrc_t mk_rsrc(const void* p, unsigned bytes) {
  return __builtin_amdgcn_make_buffer_rsrc((void*)p, 0, bytes, 0x00020000);
}
template <int AUX>
DN void glds16(rsrc_t r, char* lds, int voff, int soff) {
  __builtin_amdgcn_raw_ptr_buffer_load_lds(r, (LDS_AS void*)lds, 16, voff, soff, 0, AUX);
}
DN void bar() {
  asm volatile("s_waitcnt lgkmcnt(0)" ::: "memory");
  __builtin_amdgcn_s_barrier();
  asm volatile("" ::: "memory");
  SB();
}
template <class T> DN T lds_ld(const char* p) { return *(volatile LDS_AS T*)(LDS_AS char*)p; }
DN void lds_st(char* p, unsigned v) {
  asm volatile("" ::: "memory");
  *(volatile LDS_AS unsigned*)(LDS_AS char*)p = v;
  asm volatile("" ::: "memory");
}
DN unsigned vmin(u32x2 v) { return v[0] < v[1] ? v[0] : v[1]; }
DN unsigned vmin(u32x4 v) {
  const unsigned a = v[0] < v[1] ? v[0] : v[1], b = v[2] < v[3] ? v[2] : v[3];
  return a < b ? a : b;
}
DN unsigned vmin(unsigned v) { return v; }
template <class T, int SLP = 1> DN void lds_wait(const char* p, unsigned n, T first) {
  unsigned spins = 0;
  while (__builtin_amdgcn_readfirstlane(vmin(first)) < n && spins < 64u * SPIN_MAX) {
    if (SLP) __builtin_amdgcn_s_sleep(SLP);
    first = lds_ld<T>(p);
    ++spins;
  }
  asm volatile("" ::: "memory");
}
template <int N, int OFF2>
DN void lds_wait2(unsigned addr, unsigned f0, unsigned f1) {
  unsigned s0, s1, cnt;
  asm volatile(
      "s_mov_b32 %4, 0\n"
      "1:\n\t"
      "v_readfirstlane_b32 %2, %0\n\t"
      "v_readfirstlane_b32 %3, %1\n\t"
      "s_add_u32 %4, %4, 1\n\t"
      "s_min_u32 %2, %2, %3\n\t"
      "s_cmp_ge_u32 %2, %6\n\t"
      "s_cbranch_scc1 2f\n\t"
      "s_bitcmp1_b32 %4, 24\n\t"
      "s_cbranch_scc1 2f\n\t"
      "s_sleep 1\n\t"
      "ds_read_b32 %0, %5\n\t"
      "ds_read_b32 %1, %5 offset:%7\n\t"
      "s_waitcnt lgkmcnt(0)\n\t"
      "s_branch 1b\n"
      "2:"
      : "+v"(f0), "+v"(f1), "=&s"(s0), "=&s"(s1), "=&s"(cnt)
      : "v"(addr), "i"(N), "i"(OFF2)
      : "scc", "memory");
}
DN unsigned lds_addr(const char* p) { return (unsigned)(uintptr_t)(LDS_AS const char*)p; }
DN void tr4(unsigned b, u32x4 (&v)[4]) {
  u32x2 t[8];
  asm volatile(
      "ds_read_b64_tr_b16 %0, %8\n\tds_read_b64_tr_b16 %1, %8 offset:576\n\t"
      "ds_read_b64_tr_b16 %2, %8 offset:32\n\tds_read_b64_tr_b16 %3, %8 offset:608\n\t"
      "ds_read_b64_tr_b16 %4, %8 offset:64\n\tds_read_b64_tr_b16 %5, %8 offset:640\n\t"
      "ds_read_b64_tr_b16 %6, %8 offset:96\n\tds_read_b64_tr_b16 %7, %8 offset:672\n\t"
      "s_waitcnt lgkmcnt(0)"
      : "=&v"(t[0]), "=&v"(t[1]), "=&v"(t[2]), "=&v"(t[3]), "=&v"(t[4]), "=&v"(t[5]), "=&v"(t[6]), "=&v"(t[7])
      : "v"(b)
      : "memory");
#pragma unroll
  for (int i = 0; i < 4; ++i) v[i] = __builtin_shufflevector(t[2 * i], t[2 * i + 1], 0, 1, 2, 3);
}
DN bf16x8 tr_frag(const char* p0, const char* p1) {
  s16x4 lo = __builtin_amdgcn_ds_read_tr16_b64_v4i16((LDS_AS s16x4*)p0);
  s16x4 hi = __builtin_amdgcn_ds_read_tr16_b64_v4i16((LDS_AS s16x4*)p1);
  return __builtin_shufflevector(lo, hi, 0, 1, 2, 3, 4, 5, 6, 7);
}
DN u64 flag_ld(rsrc_t rws, unsigned off) {
  asm volatile("" ::: "memory");
  const u32x2 v = __builtin_amdgcn_raw_buffer_load_b64(rws, off, 0, SC1);
  asm volatile("" ::: "memory");
  return (u64)v[0] | ((u64)v[1] << 32);
}
DN void flag_st(rsrc_t rws, unsigned off, u64 v) {
  asm volatile("" ::: "memory");
  __builtin_amdgcn_raw_buffer_store_b64((u32x2){(unsigned)v, (unsigned)(v >> 32)}, rws, off, 0, SC1);
  asm volatile("" ::: "memory");
}
DN void flag_wait(rsrc_t rws, unsigned off, bool need, u64 tag) {
  unsigned spins = 0;
  for (;;) {
    if (need) need = flag_ld(rws, off) != tag;
    if (__builtin_amdgcn_ballot_w64(need) == 0 || ++spins >= SPIN_MAX) break;
    __builtin_amdgcn_s_sleep(8);
  }
  asm volatile("" ::: "memory");
}
DN u32x4 ld16(rsrc_t r, int off) { return __builtin_amdgcn_raw_buffer_load_b128(r, off, 0, 0); }
DN u32x2 ld8(rsrc_t r, int off) { return __builtin_amdgcn_raw_buffer_load_b64(r, off, 0, 0); }
DN u16 ld2(const u16* p, unsigned n, int idx) {
  return __builtin_amdgcn_raw_buffer_load_b16(mk_rsrc(p, n * 2), idx * 2, 0, 0);
}
DN float exp_acc(float x) {
  const float l2e = 1.44269502162933349609375f, l2e_lo = 1.925963033500011e-08f;
  const float t = x * l2e;
  const float e = __builtin_fmaf(x, l2e_lo, __builtin_fmaf(x, l2e, -t));
  const float r = __builtin_amdgcn_exp2f(t);
  return __builtin_fmaf(r, e * 0.693147180559945f, r);
}
DN void sincos_acc(float x, float& s, float& c) {
  const float k = __builtin_rintf(x * 0.636619772367581f);
  float r = __builtin_fmaf(k, -1.57079601287841796875f, x);
  r = __builtin_fmaf(k, -3.1391647326017846353352069854736328125e-7f, r);
  r = __builtin_fmaf(k, -5.390302529957764765544681040410068817436695098876953125e-15f, r);
  const float z = r * r;
  float ps = __builtin_fmaf(z, 2.7183114939898219064e-6f, -1.98393348360966317347e-4f);
  ps = __builtin_fmaf(z, ps, 8.3333293858894631756e-3f);
  ps = __builtin_fmaf(z, ps, -1.66666666416265235595e-1f);
  const float sn = __builtin_fmaf(r * z, ps, r);
  float pc = __builtin_fmaf(z, 2.43904487962774090654e-5f, -1.38867637746099294692e-3f);
  pc = __builtin_fmaf(z, pc, 4.16666233237390631894e-2f);
  pc = __builtin_fmaf(z, pc, -4.99999997251031003120e-1f);
  const float cs = __builtin_fmaf(z, pc, 1.f);
  const int q = (int)k;
  const float s0 = (q & 1) ? cs : sn, c0 = (q & 1) ? sn : cs;
  s = (q & 2) ? -s0 : s0;
  c = ((q + 1) & 2) ? -c0 : c0;
}
DN void chan_consts(u16 ldt, u16 lre, u16 lim, cpx& lam, cpx& coef) {
  const float dt = rbf(exp_acc(bf2f(ldt)));
  const float lr = bf2f(lre), li = bf2f(lim);
  const float e = exp_acc(dt * lr);
  float sn, cs;
  sincos_acc(dt * li, sn, cs);
  lam = {mul_(e, cs), mul_(e, sn)};
  const float nr = lam.re - 1.f, ni = lam.im, den = fma_(lr, lr, mul_(li, li));
  coef = {fma_(nr, lr, mul_(ni, li)) / den, fnma_(nr, li, mul_(ni, lr)) / den};
}
DN f32x2 pk_fma(f32x2 a, f32x2 b, f32x2 c) {
  f32x2 d;
  asm("v_pk_fma_f32 %0, %1, %2, %3" : "=v"(d) : "v"(a), "v"(b), "v"(c));
  return d;
}
DN f32x2 pk_mul(f32x2 a, f32x2 b) {
  f32x2 d;
  asm("v_pk_mul_f32 %0, %1, %2" : "=v"(d) : "v"(a), "v"(b));
  return d;
}
template <int HI, int NEG>
DN f32x2 pk_fma_b(f32x2 k, f32x2 b, f32x2 c) {
  f32x2 d;
  if (HI && NEG) asm("v_pk_fma_f32 %0, %1, %2, %3 op_sel:[1,0,0] neg_lo:[1,0,0] neg_hi:[1,0,0]" : "=v"(d) : "v"(k), "v"(b), "v"(c));
  else if (HI) asm("v_pk_fma_f32 %0, %1, %2, %3 op_sel:[1,0,0]" : "=v"(d) : "v"(k), "v"(b), "v"(c));
  else if (NEG) asm("v_pk_fma_f32 %0, %1, %2, %3 op_sel_hi:[0,1,1] neg_lo:[1,0,0] neg_hi:[1,0,0]" : "=v"(d) : "v"(k), "v"(b), "v"(c));
  else asm("v_pk_fma_f32 %0, %1, %2, %3 op_sel_hi:[0,1,1]" : "=v"(d) : "v"(k), "v"(b), "v"(c));
  return d;
}
template <int HI, int NEG>
DN f32x2 pk_mul_b(f32x2 k, f32x2 b) {
  f32x2 d;
  if (HI && NEG) asm("v_pk_mul_f32 %0, %1, %2 op_sel:[1,0] neg_lo:[1,0] neg_hi:[1,0]" : "=v"(d) : "v"(k), "v"(b));
  else if (HI) asm("v_pk_mul_f32 %0, %1, %2 op_sel:[1,0]" : "=v"(d) : "v"(k), "v"(b));
  else if (NEG) asm("v_pk_mul_f32 %0, %1, %2 op_sel_hi:[0,1] neg_lo:[1,0] neg_hi:[1,0]" : "=v"(d) : "v"(k), "v"(b));
  else asm("v_pk_mul_f32 %0, %1, %2 op_sel_hi:[0,1]" : "=v"(d) : "v"(k), "v"(b));
  return d;
}
DN cpx to_c(f32x2 v) { return {v[0], v[1]}; }
DN unsigned relu_pk(unsigned v) {
  typedef __attribute__((ext_vector_type(2))) short s16x2;
  const s16x2 z = {0, 0};
  return __builtin_bit_cast(unsigned, __builtin_elementwise_max(__builtin_bit_cast(s16x2, v), z));
}
DN f32x2 lo2(f32x4 v) { return __builtin_shufflevector(v, v, 0, 1); }
DN f32x2 hi2(f32x4 v) { return __builtin_shufflevector(v, v, 2, 3); }
DN float sum8(u32x4 v) {
  return ((lo_f(v[0]) + hi_f(v[0])) + (lo_f(v[1]) + hi_f(v[1]))) + ((lo_f(v[2]) + hi_f(v[2])) + (lo_f(v[3]) + hi_f(v[3])));
}
DN float row_even(float v) {
  return as_f(__builtin_amdgcn_permlane16_swap(as_u(v), as_u(v), false, false)[0]);
}
DN float row1_all(float v) {
  const unsigned o = __builtin_amdgcn_permlane16_swap(as_u(v), as_u(v), false, false)[1];
  return as_f(__builtin_amdgcn_permlane32_swap(o, o, false, false)[0]);
}
DN float row_sum(float v) {
  const auto a = __builtin_amdgcn_permlane16_swap(as_u(v), as_u(v), false, false);
  const float t = as_f(a[0]) + as_f(a[1]);
  const auto b = __builtin_amdgcn_permlane32_swap(as_u(t), as_u(t), false, false);
  return as_f(b[0]) + as_f(b[1]);
}
DN float red8(float v, int l) {
  v += lane_get(v, l ^ 1);
  v += lane_get(v, l ^ 2);
  v += lane_get(v, l ^ 4);
  return v;
}
DN char* job_tile(char* sm, int j) { return sm + (j < 8 ? j * JOBSZ : LD_AUX + (j - 8) * JOBSZ); }
DN int kf_start(int w) { return w < 2 ? NF * w : 2 * NF + (NF - 1) * (w - 2); }

template <bool BU>
DN void p1_gemm(const Args& a, char* sm, int ti, int tj, int w, int l) {
  const int wm = w >> 1, wn = w & 1, r = l & 15, g = l >> 4, jb = tj - 9;
  int xo[2], wo[2][2];
#pragma unroll
  for (int kk = 0; kk < 2; ++kk) {
    xo[kk] = (64 * wm + r) * 128 + (((4 * kk + g) ^ (r & 7)) << 4);
    if (BU) {
      wo[kk][0] = 16384 + (96 * wn + r) * 128 + (((4 * kk + g) ^ (r & 7)) << 4);
      wo[kk][1] = 0;
    } else {
      const int q = r >> 2, p = r & 3, f = (q >> 1) | ((g & 1) << 1);
      const int base = 16384 + (4 * kk + g) * 3072 + q * 128 + 8 * p;
      wo[kk][0] = base + (((2 * wn) ^ f) << 5);
      wo[kk][1] = base + (((2 * wn + 1) ^ f) << 5);
    }
  }
  bf16x8 xf[2][4], wf[2][6];
  auto rd = [&](int s, int h, int i) {
    const char* slot = sm + (s % 3) * SLOT;
    if (i >= 1 && i <= 4) {
      xf[h][i - 1] = *(const bf16x8*)(slot + xo[h] + (i - 1) * 2048);
    } else {
      const int nt = i == 0 ? 0 : i - 4;
      if (BU) wf[h][nt] = *(const bf16x8*)(slot + wo[h][0] + nt * 2048);
      else {
        const char* b = slot + wo[h][nt & 1] + (nt >> 1) * 1024;
        wf[h][nt] = tr_frag(b, b + 512);
      }
    }
  };
  f32x4 acc[6][4];
  const f32x4 z4 = {0.f, 0.f, 0.f, 0.f};
  u32x2 fl = {0u, 0u};
  auto half = [&](auto sc, int h) {
    constexpr int s = decltype(sc)::value;
#pragma unroll
    for (int n = 0; n < 6; ++n)
#pragma unroll
      for (int m = 0; m < 4; ++m) {
        const int idx = 4 * n + m;
        if (h == 0) {
          if (idx < 10) rd(s, 1, idx);
          if (idx == 10) lds_st(sm + SY_FREE + 4 * w, s + 1);
          if (idx == 16 + (RD_AT > 7 ? 7 : RD_AT)) fl = lds_ld<u32x2>(sm + (s < 9 ? SY_FULL : SY_GATH));
        } else {
          if (idx == RD_AT) {
            if (s < 9) lds_wait2<s + 2, 0>(lds_addr(sm + SY_FULL + 4 * ((s + 1) & 1)), fl[(s + 1) & 1], fl[(s + 1) & 1]);
            else lds_wait2<1, 4>(lds_addr(sm + SY_GATH), fl[0], fl[1]);
            SB();
          }
          if (s < 9 && idx >= RD_AT && idx < RD_AT + 10) rd(s + 1, 0, idx - RD_AT);
        }
        const bool first = s == 0 && h == 0;
        if (BU) acc[n][m] = MFMA(xf[h][m], wf[h][n], first ? z4 : acc[n][m]);
        else acc[n][m] = MFMA(wf[h][n], xf[h][m], first ? z4 : acc[n][m]);
        SB();
      }
  };
  bar();
  lds_wait2<1, 0>(lds_addr(sm + SY_FULL), 0u, 0u);
#pragma unroll
  for (int i = 0; i < 10; ++i) rd(0, 0, i);
  SB();
  auto stage = [&](auto sc) {
    half(sc, 0);
    half(sc, 1);
  };
  stage(IC<0>{}); stage(IC<1>{}); stage(IC<2>{}); stage(IC<3>{}); stage(IC<4>{});
  stage(IC<5>{}); stage(IC<6>{}); stage(IC<7>{}); stage(IC<8>{});
  const float* RM = (const float*)(sm + LD_RM);
  const float* RI = (const float*)(sm + LD_RI);
  const float* CS = (const float*)(sm + LD_CS);
  const int rt = 2 * ti + wm;
  const rsrc_t rws = mk_rsrc(a.ws, WS_END);
  if (!BU) {
    half(IC<9>{}, 0);
    lds_wait2<1, 4>(lds_addr(sm + SY_GATH), fl[0], fl[1]);
    SB();
    f32x2 mu[4];
    f32x4 cc[3][2];
#pragma unroll
    for (int m = 0; m < 4; ++m) {
      const float t = -RM[64 * wm + 16 * m + r];
      mu[m] = (f32x2){t, t};
    }
#pragma unroll
    for (int pc = 0; pc < 3; ++pc) {
      cc[pc][0] = *(const f32x4*)(CS + 64 * pc + 32 * wn + 4 * g);
      cc[pc][1] = *(const f32x4*)(CS + 64 * pc + 32 * wn + 16 + 4 * g);
    }
    auto fin = [&](int pc, int m) {
      const f32x4 v0 = acc[2 * pc][m] + cc[pc][0] * mu[m][0], v1 = acc[2 * pc + 1][m] + cc[pc][1] * mu[m][0];
      const u32x4 o = {relu_pk(pk_bf16(v0[0], v0[1])), relu_pk(pk_bf16(v0[2], v0[3])), relu_pk(pk_bf16(v1[0], v1[1])),
                       relu_pk(pk_bf16(v1[2], v1[3]))};
      __builtin_amdgcn_raw_buffer_store_b128(o, rws, WS_H1F + ((rt * KSF + 6 * tj + 2 * pc + wn) * 4 + m) * 1024 + l * 16, 0,
                                             SC1);
    };
#pragma unroll
    for (int pc = 0; pc < 4; ++pc)
#pragma unroll
      for (int m = 0; m < 4; ++m) {
        if (pc < 3) {
          acc[2 * pc][m] = MFMA(wf[1][2 * pc], xf[1][m], acc[2 * pc][m]);
          acc[2 * pc + 1][m] = MFMA(wf[1][2 * pc + 1], xf[1][m], acc[2 * pc + 1][m]);
        }
        if (pc > 0) fin(pc - 1, m);
        SB();
      }
  } else {
    half(IC<9>{}, 0);
    lds_wait2<1, 4>(lds_addr(sm + SY_GATH), fl[0], fl[1]);
    SB();
  }

  const u64 tag = *(const u64*)(sm + SY_TAG);
  if (!BU) {
    lds_st(sm + SY_FREE + 4 * w, 11u);
    WAIT_VM(0);
    if (l == 0) flag_st(rws, WS_FAF + (rt * 32 + 2 * tj + wn) * 8, tag);
  } else {
    f32x2 rm[8], ri[8];
#pragma unroll
    for (int m = 0; m < 4; ++m) {
      const f32x4 t0 = *(const f32x4*)(RM + 64 * wm + 16 * g + 4 * m), t1 = *(const f32x4*)(RI + 64 * wm + 16 * g + 4 * m);
      rm[2 * m] = lo2(t0); rm[2 * m + 1] = hi2(t0); ri[2 * m] = lo2(t1); ri[2 * m + 1] = hi2(t1);
    }
    f32x2 lam[3], cs[3], hre[3][8], him[3][8];
    cpx l8[3], l16[3], l32[3];
#pragma unroll
    for (int c = 0; c < 3; ++c) {
      const int ctl = 3 * wn + c;
      const char* lp = sm + LD_LAM + (16 * ctl + r) * 48;
      const f32x4 t0 = *(const f32x4*)lp, t1 = *(const f32x4*)(lp + 16);
      const f32x2 t2 = *(const f32x2*)(lp + 32);
      lam[c] = lo2(t0); l8[c] = to_c(lo2(t1)); l16[c] = to_c(hi2(t1)); l32[c] = to_c(t2);
      cs[c] = (f32x2){CS[32 * ctl + r], CS[32 * ctl + 16 + r]};
    }
    SB();
#pragma unroll
    for (int cc = 0; cc < 4; ++cc)
#pragma unroll
      for (int k = 0; k < 8; ++k) {
        if (cc < 3) acc[2 * cc + (k & 1)][k >> 1] = MFMA(xf[1][k >> 1], wf[1][2 * cc + (k & 1)], acc[2 * cc + (k & 1)][k >> 1]);
        if (cc > 0) {
          const int c = cc - 1;
          const f32x4 ar = acc[2 * c][k >> 1], ai = acc[2 * c + 1][k >> 1];
          f32x2 ur = __builtin_elementwise_fma((k & 1) ? hi2(ar) : lo2(ar), ri[k], pk_mul_b<0, 1>(cs[c], rm[k]));
          f32x2 ui = __builtin_elementwise_fma((k & 1) ? hi2(ai) : lo2(ai), ri[k], pk_mul_b<1, 1>(cs[c], rm[k]));
          if (k > 0) {
            ur = pk_fma_b<0, 0>(lam[c], hre[c][k - 1], pk_fma_b<1, 1>(lam[c], him[c][k - 1], ur));
            ui = pk_fma_b<0, 0>(lam[c], him[c][k - 1], pk_fma_b<1, 0>(lam[c], hre[c][k - 1], ui));
          }
          hre[c][k] = ur;
          him[c][k] = ui;
        }
        if (cc < 3) SB();
      }
    const u32x4 fr = lds_ld<u32x4>(sm + SY_FREE);
    const int nct = (jb == 6 && wn == 1) ? 1 : 3;
    SB();
    cpx ea[3], ex[3];
#pragma unroll
    for (int c = 0; c < 3; ++c) {
      const int ct = 6 * jb + 3 * wn + c;
      ea[c] = {hre[c][7][0], him[c][7][0]};
      const cpx v = c_fma(l8[c], ea[c], cpx{hre[c][7][1], him[c][7][1]});
      const cpx ev0 = {row_even(v.re), row_even(v.im)};
      const cpx p = c_fma(l16[c], ev0, v);
      const cpx b = {row1_all(p.re), row1_all(p.im)};
      const cpx tot = c_fma(l32[c], b, p);
      const u32x4 ev = {as_u(tot.re), (unsigned)tag, as_u(tot.im), (unsigned)(tag >> 32)};
      __builtin_amdgcn_raw_buffer_store_b128(ev, rws, (c < nct && g == 3) ? WS_E64 + ((ct * 32 + rt) * 16 + r) * 16 : WS_END,
                                             0, SC1);
      ex[c] = c_sel(g == 3, c_fma(l16[c], b, ev0), c_sel(g == 2, b, ev0));
    }
    SB();
    lds_wait2<10, 4>(lds_addr(sm + SY_FREE), fr[0], fr[1]);
    lds_wait2<10, 4>(lds_addr(sm + SY_FREE + 8), fr[2], fr[3]);
#pragma unroll
    for (int c = 0; c < 3; ++c) {
      char* jt = job_tile(sm, 3 * w + c);
#pragma unroll
      for (int q = 0; q < 2; ++q) {
        u32x4 o0, o1;
#pragma unroll
        for (int i = 0; i < 4; ++i) {
          o0[i] = pk_bf16(hre[c][4 * q + i][0], hre[c][4 * q + i][1]);
          o1[i] = pk_bf16(him[c][4 * q + i][0], him[c][4 * q + i][1]);
        }
        *(u32x4*)(jt + q * 1024 + l * 16) = o0;
        *(u32x4*)(jt + (2 + q) * 1024 + l * 16) = o1;
      }
      ex[c] = c_sel(g == 0, cpx{0.f, 0.f}, ex[c]);
      *(f32x4*)(jt + 4096 + l * 16) = (f32x4){ex[c].re, ex[c].im, ea[c].re, ea[c].im};
    }
    lds_st(sm + SY_FREE + 4 * w, 11u);
    cpx l256[3], fac[3], H[3];
    const cpx one = {1.f, 0.f};
    const int ep = (rt - 1 - g) & 3;
#pragma unroll
    for (int c = 0; c < 3; ++c) {
      const cpx l64 = c_mul(l32[c], l32[c]), l128 = c_mul(l64, l64);
      l256[c] = c_mul(l128, l128);
      fac[c] = c_mul(c_sel(ep & 1, l64, one), c_sel(ep & 2, l128, one));
      asm volatile("" : "+v"(l256[c].re), "+v"(l256[c].im), "+v"(fac[c].re), "+v"(fac[c].im));
      H[c] = {0.f, 0.f};
    }
    __builtin_amdgcn_s_sleep(2);
    u32x4 e[3][8];
    for (unsigned spins = 0;; ++spins) {
      unsigned bad = 0u;
      asm volatile("" ::: "memory");
#pragma unroll
      for (int i = 0; i < 8; ++i)
#pragma unroll
        for (int c = 0; c < 3; ++c)
          e[c][i] = __builtin_amdgcn_raw_buffer_load_b128(
              rws, (4 * i + g < rt && c < nct) ? WS_E64 + (((6 * jb + 3 * wn + c) * 32 + 4 * i + g) * 16 + r) * 16 : WS_END,
              0, SC1);
#pragma unroll
      for (int i = 0; i < 8; ++i) {
        unsigned b = 0u;
#pragma unroll
        for (int c = 0; c < 3; ++c)
          b |= c < nct ? ((e[c][i][1] ^ (unsigned)tag) | (e[c][i][3] ^ (unsigned)(tag >> 32))) : 0u;
        bad |= 4 * i + g < rt ? b : 0u;
      }
      if (__builtin_amdgcn_ballot_w64(bad != 0u) == 0 || spins >= SPIN_MAX) break;
      __builtin_amdgcn_s_sleep(8);
    }
#pragma unroll
    for (int i = 0; i < 8; ++i)
#pragma unroll
      for (int c = 0; c < 3; ++c)
        H[c] = c_sel(4 * i + g < rt, c_fma(l256[c], H[c], cpx{as_f(e[c][i][0]), as_f(e[c][i][2])}), H[c]);
#pragma unroll
    for (int c = 0; c < 3; ++c) H[c] = c_mul(fac[c], H[c]);
#pragma unroll
    for (int c = 0; c < 3; ++c) H[c] = {row_sum(H[c].re), row_sum(H[c].im)};
#pragma unroll
    for (int c = 0; c < 3; ++c) *(f32x2*)(sm + LD_CAR + ((3 * w + c) * 16 + r) * 8) = (f32x2){H[c].re, H[c].im};
    lds_st(sm + SY_CAR + 4 * w, 1u);
  }
}

DN void p2_gemm(const Args& a, char* sm, int rt, int cg, int w, int l) {
  const int kf0 = kf_start(w), nf = w < 2 ? NF : NF - 1;
  const rsrc_t raf = mk_rsrc(a.ws + WS_H1F + (rt * KSF + kf0) * 4096, nf * 4096);
  const rsrc_t rbf_ = mk_rsrc(a.ws + WS_W2F + (cg * KSF + kf0) * 5120, nf * 5120);
  const rsrc_t ray = mk_rsrc(a.ws + WS_HF + (rt * KSY + NY * w) * 4096, NY * 4096);
  const rsrc_t rby = mk_rsrc(a.ws + WS_CF + (cg * KSY + NY * w) * 5120, NY * 5120);
  f32x4 acc[5][4];
  bf16x8 fa[PDA][4], fb[PD][5];
  const int vo = l * 16;
  char* wreg = sm + LD_P2 + w * WREG;
  auto from_lds = [](int k) { return k < 12 && k % 3 == 1; };
  auto ld_b = [&](int k, int j) {
    if (from_lds(k)) {
      fb[k % PD][j] = *(const bf16x8*)(wreg + (k / 3) * 5120 + j * 1024 + vo);
    } else {
      const int so = (k < NF ? k : k - NF) * 5120 + (j >> 2) * 4096;
      fb[k % PD][j] = __builtin_bit_cast(
          bf16x8, __builtin_amdgcn_raw_buffer_load_b128(k < NF ? rbf_ : rby, vo + (j & 3) * 1024, so, SC1));
    }
  };
  auto ld_a = [&](int k) {
#pragma unroll
    for (int i = 0; i < 4; ++i)
      fa[k % PDA][i] = __builtin_bit_cast(
          bf16x8, __builtin_amdgcn_raw_buffer_load_b128(k < NF ? raf : ray, vo + i * 1024,
                                                        (k < NF ? k : k - NF) * 4096, SC1));
  };
  char* red = wreg + (l & 15) * RED_ROW + (l >> 4) * 16;
  auto dump = [&](int j) {
#pragma unroll
    for (int i = 0; i < 4; ++i) *(f32x4*)(red + i * 16 * RED_ROW + j * 64) = acc[j][i];
  };
  {
    const unsigned pt = lds_ld<unsigned>(sm + SY_PTOK);
    lds_wait2<1, 0>(lds_addr(sm + SY_PTOK), pt, pt);
  }
#pragma unroll
  for (int k = 0; k < PD; ++k)
    if (!from_lds(k)) {
#pragma unroll
      for (int j = 0; j < 5; ++j) ld_b(k, j);
    }
  SB();
  {
    const u32x4 go = lds_ld<u32x4>(sm + SY_P2GO);
    lds_wait2<1, 4>(lds_addr(sm + SY_P2GO), go[0], go[1]);
    lds_wait2<1, 0>(lds_addr(sm + SY_P2GO + 8), go[2], go[2]);
  }
  SB();
#pragma unroll
  for (int k = 0; k < PDA; ++k) {
    ld_a(k);
    if (k < PD && from_lds(k)) {
#pragma unroll
      for (int j = 0; j < 5; ++j) ld_b(k, j);
    }
  }
  SB();
  const f32x4 z4 = {0.f, 0.f, 0.f, 0.f};
  unsigned yr = 0u;
#pragma unroll
  for (int k = 0; k < NF + NY; ++k) {
    const bool first = (k == 0 || k == NF);
    if (k + PD == NF) {
      lds_wait2<1, 0>(lds_addr(sm + SY_YRDY), yr, yr);
      SB();
    }
#pragma unroll
    for (int j = 0; j < 5; ++j) {
#pragma unroll
      for (int i = 0; i < 4; ++i) acc[j][i] = MFMA(fb[k % PD][j], fa[k % PDA][i], first ? z4 : acc[j][i]);
      SB();
      if (k + PD < NF + NY) ld_b(k + PD, j);
      if ((k == NF - 1 || k == NF + NY - 1) && j > 0) dump(j - 1);
      SB();
    }
    if (k == YPOLL_AT) lds_st(sm + SY_YRDY + 4, 1u);
    if (k + PD == NF - 2) yr = lds_ld<unsigned>(sm + SY_YRDY);
#pragma unroll
    for (int kn = k + 1; kn <= k + PDA; ++kn) {
      const int due = (kn >= NF && kn - PDA < NF - PD) ? NF - PD : kn - PDA;
      if (kn < NF + NY && due == k) ld_a(kn);
    }
    SB();
    if (k == NF - 1) {
      dump(4);
      bar();
    }
    if (k == NF + NY - 2) bar();
  }
  dump(4);
  bar();
}

struct Early { u32x2 xv[5], dv[5]; };
DN void early_loads(const Args& a, int rt, int cg, int hw, int l, Early& y) {
#pragma unroll
  for (int e = 0; e < 5; ++e) {
    const int idx = hw * 64 + l + 256 * e, row = idx / 20, c4 = idx - row * 20;
    y.xv[e] = ld8(mk_rsrc(a.x, L_ * D_ * 2), ((rt * 64 + row) * D_ + cg * 80 + c4 * 4) * 2);
    y.dv[e] = ld8(mk_rsrc(a.dsk, D_ * 2), (cg * 80 + c4 * 4) * 2);
  }
}
DN void p1_loader(const Args& a, char* sm, int ti, int tj, int rt, int cg, int lw, int l, Early& y) {
  const bool bu = tj >= 9;
  const int l3 = l >> 3, l7 = l & 7, jb = tj - 9;
  const rsrc_t rx = mk_rsrc(a.x + (size_t)ti * 128 * D_, 128 * D_ * 2);
  const int vx = (bu ? (16 * (l3 >> 2) + 8 * (l3 & 1) + ((l3 >> 1) & 1)) : l3) * (D_ * 2) + ((l7 ^ l3) << 4);
  const rsrc_t rw0 = mk_rsrc(bu ? a.bre : a.w1, bu ? P_ * D_ * 2 : D_ * F_ * 2);
  const rsrc_t rw1 = mk_rsrc(bu ? a.bim : a.w1, bu ? P_ * D_ * 2 : D_ * F_ * 2);
  const int f0 = (l3 >> 1) & 1, fb_ = l3 * (F_ * 2) + tj * 384 + (l7 & 1) * 16;
  const int sb_ = (96 * jb + l3) * (D_ * 2) + ((l7 ^ l3) << 4);
  const int vw0 = bu ? sb_ : fb_ + (((l7 >> 1) ^ f0) << 5);
  const int vw1 = bu ? sb_ : fb_ + (((l7 >> 1) ^ (f0 | 2)) << 5);
  const int kstep = bu ? 128 : 64 * F_ * 2;
  auto stage = [&](int s, int slot_i) {
    char* slot = sm + slot_i * SLOT;
#pragma unroll
    for (int p = 0; p < 16; ++p) {
      const int q = p >> 2, v = p & 3;
      const int rowoff = bu ? (64 * (q >> 1) + 4 * (q & 1) + 32 * (v & 1) + 2 * (v >> 1)) : (32 * q + 8 * v);
      glds16<0>(rx, slot + p * 1024, vx, rowoff * (D_ * 2) + s * 128);
      if (p < 12) {
#pragma unroll
        for (int e = 0; e < 2; ++e) {
          const int j = 2 * p + e;
          const int so = bu ? ((j >> 2) * 16 + (j & 1) * 8) * (D_ * 2) : (j / 3) * 8 * (F_ * 2) + (j % 3) * 128;
          glds16<0>(((j >> 1) & 1) ? rw1 : rw0, slot + 16384 + j * 1024, ((j / 3) & 1) ? vw1 : vw0, so + s * kstep);
        }
      }
    }
  };
  const rsrc_t rb = mk_rsrc(a.ws + WS_W2F + cg * KSF * 5120, KSF * 5120);
  auto prefetch = [&](int g0, int n) {
#pragma unroll 1
    for (int gi = g0 + lw; gi < g0 + n; gi += 2) {
      const int gw = gi >> 2, gq = gi & 3;
      const int so = (kf_start(gw) + 3 * gq + 1) * 5120;
#pragma unroll
      for (int j = 0; j < 5; ++j) glds16<SC1>(rb, sm + LD_P2 + gw * WREG + gq * 5120 + j * 1024, l * 16, so + j * 1024);
    }
  };
  bar();
  __builtin_amdgcn_s_setprio(3);
  int slot_i = lw;
#pragma unroll 1
  for (int t = lw; t < 10; t += 2) {
    if (t >= 3) lds_wait<u32x4, 1>(sm + SY_FREE, t - 2, lds_ld<u32x4>(sm + SY_FREE));
    stage(t, slot_i);
    WAIT_VM(0);
    lds_st(sm + SY_FULL + 4 * lw, t + 1);
    slot_i = slot_i == 0 ? 2 : slot_i - 1;
  }
  lds_wait(sm + SY_PTOK, 1u, lds_ld<unsigned>(sm + SY_PTOK));
  lds_wait(sm + SY_FREE, 8u, lds_ld<u32x4>(sm + SY_FREE));
  prefetch(0, 7);
  lds_wait(sm + SY_FREE, 9u, lds_ld<u32x4>(sm + SY_FREE));
  prefetch(7, 9);
  early_loads(a, rt, cg, lw, l, y);
  WAIT_VM(0);
  lds_st(sm + SY_P2GO + 4 * lw, 1u);
}

template <bool BU>
DN void p1_side(const Args& a, char* sm, int ti, int tj, int rt, int cg, int sw, int l) {
  const int jb = tj - 9, r = l & 15, g = l >> 4, l3 = l >> 3, l7 = l & 7;
  const int bl = ti * 9 + tj, pcg = bl & 7, pq = bl >> 3;
  const rsrc_t rws = mk_rsrc(a.ws, WS_END);
  if (sw == 0 && l < 32) lds_st(sm + LD_SYNC + 4 * l, l == 19 ? 1u : 0u);
  bar();
  SB();
  u32x4 va[20];
  if (sw == 0) {
    const rsrc_t rs = mk_rsrc(a.x, L_ * D_ * 2);
#pragma unroll
    for (int i = 0; i < 10; ++i) va[i] = ld16(rs, (ti * 128 + 8 * tj + l3) * (D_ * 2) + l7 * 16 + i * 128);
  } else if (BU) {
    const int off = l3 < 6 ? (96 * jb + 6 * ti + l3) * (D_ * 2) + l7 * 16 : P_ * D_ * 2;
#pragma unroll
    for (int h = 0; h < 2; ++h) {
      const rsrc_t rs = mk_rsrc(h ? a.bim : a.bre, P_ * D_ * 2);
#pragma unroll
      for (int i = 0; i < 10; ++i) va[10 * h + i] = ld16(rs, off + i * 128);
    }
  } else {
    const rsrc_t rs = mk_rsrc(a.w1, D_ * F_ * 2);
    const int c = l % 24, rg = l / 24;
    const int off = l < 48 ? (40 * ti + 20 * rg) * (F_ * 2) + 384 * tj + c * 16 : D_ * F_ * 2;
#pragma unroll
    for (int i = 0; i < 20; ++i) va[i] = ld16(rs, off + i * (F_ * 2));
  }
  const u64 ep = flag_ld(rws, WS_EP);
  u16 c_ldt = 0, c_lre = 0, c_lim = 0;
  if (BU) {
    int p = 96 * jb + 48 * sw + (l < 48 ? l : 0);
    p = p < P_ ? p : P_ - 1;
    c_ldt = ld2(a.ldt, P_, p); c_lre = ld2(a.lre, P_, p); c_lim = ld2(a.lim, P_, p);
  }
  SB();
  auto piece = [&](int e) { return sw ? 10 + (e % 5) : e; };
  WAIT_VM(0);
#pragma unroll
  for (int i = 0; i < 20; ++i) asm volatile("" : "+v"(va[i]));
  if (!BU) {
    const rsrc_t r2 = mk_rsrc(a.w2, F_ * D_ * 2);
#pragma unroll
    for (int e = 0; e < 10; ++e) {
      const int pc = piece(e);
      const int q = pc * 64 + l, row = q / 10, ch = q - row * 10;
      if (e < 5 || sw == 0)
        glds16<0>(r2, sm + LD_AUX + pc * 1024, (96 * pq + row) * (D_ * 2) + pcg * 160 + ch * 16, 0);
    }
  }
  SB();
  if (sw == 0) {
    float s = 0.f;
#pragma unroll
    for (int i = 0; i < 10; ++i) s += sum8(va[i]);
    const float mean = red8(s, l) * (1.f / D_);
    float q = 0.f;
#pragma unroll
    for (int i = 0; i < 10; ++i)
#pragma unroll
      for (int e = 0; e < 4; ++e) {
        const float d0 = lo_f(va[i][e]) - mean, d1 = hi_f(va[i][e]) - mean;
        q = __builtin_fmaf(d0, d0, __builtin_fmaf(d1, d1, q));
      }
    const float var = red8(q, l) * (1.f / D_);
    const float sd = rbf(__builtin_sqrtf(rbf(rbf(var) + 1e-5f)));
    const u32x2 o = {as_u(rbf(mean)), as_u(sd)};
    if (l7 == 0) __builtin_amdgcn_raw_buffer_store_b64(o, rws, WS_STAT + (ti * 128 + 8 * tj + l3) * 8, 0, SC1);
    if (l == 0) *(u64*)(sm + SY_TAG) = ep + 0x9E3779B97F4A7C15ull;
    lds_st(sm + SY_TAG + 8, 1u);
  } else if (BU) {
#pragma unroll
    for (int h = 0; h < 2; ++h) {
      float s = 0.f;
#pragma unroll
      for (int i = 0; i < 10; ++i) s += sum8(va[10 * h + i]);
      s = red8(s, l);
      const int ch = 6 * ti + l3;
      if (l7 == 0 && l3 < 6)
        __builtin_amdgcn_raw_buffer_store_b32(as_u(s), rws, WS_CSS + (jb * 192 + 32 * (ch >> 4) + 16 * h + (ch & 15)) * 4, 0,
                                              SC1);
    }
  } else {
    float s[8];
#pragma unroll
    for (int e = 0; e < 8; ++e) s[e] = 0.f;
#pragma unroll
    for (int i = 0; i < 20; ++i)
#pragma unroll
      for (int e = 0; e < 4; ++e) { s[2 * e] += lo_f(va[i][e]); s[2 * e + 1] += hi_f(va[i][e]); }
#pragma unroll
    for (int e = 0; e < 8; ++e) s[e] += lane_get(s[e], l + 24);
    if (l < 24) {
#pragma unroll
      for (int h = 0; h < 2; ++h) {
        const u32x4 o = {as_u(s[4 * h]), as_u(s[4 * h + 1]), as_u(s[4 * h + 2]), as_u(s[4 * h + 3])};
        __builtin_amdgcn_raw_buffer_store_b128(o, rws, WS_CSF + (tj * 16 + ti) * 768 + (24 * h + l) * 16, 0, SC1);
      }
    }
  }
  lds_wait(sm + SY_TAG + 8, 1u, lds_ld<unsigned>(sm + SY_TAG + 8));
  const u64 tag = *(const u64*)(sm + SY_TAG);
  WAIT_VM(0);
  if (l == 0) flag_st(rws, sw == 0 ? WS_FST + (ti * 16 + tj) * 8 : WS_FCS + (tj * 16 + ti) * 8, tag);
  u32x4 ct_[8];
  if (BU) {
    const int id = ((ti * 7 + jb) * 2 + sw) * 8;
    const rsrc_t rc = mk_rsrc(id < 800 ? a.cre : a.cim, D_ * P_ * 2);
#pragma unroll
    for (int e = 0; e < 8; ++e) ct_[e] = ld16(rc, ((id < 800 ? id : id - 800) + e) * 1024 + l * 16);
  }
  if (!BU) {
    u32x4 vc[10];
#pragma unroll
    for (int e = 0; e < 10; ++e) {
      const int pc = piece(e), ksl = pc / 5, nt = pc - 5 * ksl;
      const char* b = sm + LD_AUX + (32 * ksl + 4 * g + (r >> 2)) * 160 + nt * 32 + (r & 3) * 8;
      vc[e] = __builtin_bit_cast(u32x4, tr_frag(b, b + 16 * 160));
    }
#pragma unroll
    for (int e = 0; e < 10; ++e)
      if (e < 5 || sw == 0)
        __builtin_amdgcn_raw_buffer_store_b128(vc[e], rws, WS_W2F + (pcg * KSF + 3 * pq) * 5120 + piece(e) * 1024 + l * 16,
                                               0, SC1);
  }
  if (BU) {
    cpx lam, coef;
    chan_consts(c_ldt, c_lre, c_lim, lam, coef);
    const cpx l2 = c_mul(lam, lam), l4 = c_mul(l2, l2), l8 = c_mul(l4, l4), l16 = c_mul(l8, l8), l32 = c_mul(l16, l16);
    if (l < 48) {
      char* lp = sm + LD_LAM + (48 * sw + l) * 48;
      *(f32x4*)lp = (f32x4){lam.re, lam.im, coef.re + coef.re, coef.im + coef.im};
      *(f32x4*)(lp + 16) = (f32x4){l8.re, l8.im, l16.re, l16.im};
      *(f32x2*)(lp + 32) = (f32x2){l32.re, l32.im};
    }
  }
  WAIT_VM(0);
  if (BU) {
#pragma unroll
    for (int e = 0; e < 8; ++e) asm volatile("" ::"v"(ct_[e]));
  } else {
    if (l == 0) flag_st(rws, WS_FPT + ((pcg * 32 + pq) * 2 + sw) * 8, tag);
    lds_st(sm + SY_CAR + 4 * sw, 1u);
  }
  const unsigned fp = (sw == 0 ? WS_FST + (l < 16 ? ti : (rt >> 1)) * 128 : WS_FCS + tj * 128) + r * 8;
  lds_wait<unsigned, 8>(sm + SY_FREE, sw ? GATHER1_AT : GATHER_AT, lds_ld<unsigned>(sm + SY_FREE));
  __builtin_amdgcn_s_setprio(3);
  if (sw == 0) {
    flag_wait(rws, WS_FPT + cg * 512 + l * 8, l < 36, tag);
    lds_st(sm + SY_PTOK, 1u);
  }
  flag_wait(rws, fp, l < (sw == 0 ? 32 : 16), tag);
  u32x4 gv[16];
  if (sw == 0) {
    gv[0] = __builtin_amdgcn_raw_buffer_load_b128(rws, WS_STAT + ti * 1024 + l * 16, 0, SC1);
    const u32x2 s2 = __builtin_amdgcn_raw_buffer_load_b64(rws, WS_STAT + rt * 512 + l * 8, 0, SC1);
    *(u32x2*)(sm + LD_ST2 + l * 8) = s2;
#pragma unroll
    for (int h = 0; h < 2; ++h) {
      const float mb = as_f(gv[0][2 * h]), sd = as_f(gv[0][2 * h + 1]);
      const float ri = 1.f / sd;
      const int t = 2 * l + h;
      const int pos = BU ? ((t & ~15) + ((t & 6) << 1) + ((t & 1) << 1) + ((t >> 3) & 1)) : t;
      ((float*)(sm + LD_RM))[pos] = BU ? mb * ri : mb;
      ((float*)(sm + LD_RI))[pos] = ri;
    }
  } else if (BU) {
    gv[0] = __builtin_amdgcn_raw_buffer_load_b128(rws, WS_CSS + jb * 768 + (l < 48 ? l : 0) * 16, 0, SC1);
    if (l < 48) *(u32x4*)(sm + LD_CS + l * 16) = gv[0];
  } else {
#pragma unroll
    for (int i = 0; i < 16; ++i)
      gv[i] = __builtin_amdgcn_raw_buffer_load_b128(rws, l < 48 ? WS_CSF + (tj * 16 + i) * 768 + l * 16 : WS_END, 0, SC1);
    f32x4 t[4];
#pragma unroll
    for (int q = 0; q < 4; ++q)
      t[q] = (__builtin_bit_cast(f32x4, gv[4 * q]) + __builtin_bit_cast(f32x4, gv[4 * q + 1])) +
             (__builtin_bit_cast(f32x4, gv[4 * q + 2]) + __builtin_bit_cast(f32x4, gv[4 * q + 3]));
    if (l < 48) *(f32x4*)(sm + LD_CS + (l < 24 ? 2 * l : 2 * l - 47) * 16) = (t[0] + t[1]) + (t[2] + t[3]);
  }
  asm volatile("" ::: "memory");
  lds_st(sm + SY_GATH + 4 * sw, 1u);
  __builtin_amdgcn_s_setprio(0);
  if (!BU) {
    lds_wait(sm + SY_CAR, 1u, lds_ld<u32x2>(sm + SY_CAR));
    const int job = bl * 2 + sw, jq = job >> 3, nt = jq / 5, kg = jq - 5 * nt;
    if (job < 200) {
#pragma unroll
      for (int e = 0; e < 8; ++e) {
        const int row = 4 * (e & 3) + g;
        glds16<0>(mk_rsrc(e < 4 ? a.cre : a.cim, D_ * P_ * 2), sm + LD_AUX + sw * 8192 + e * 1024,
                  (80 * (job & 7) + 16 * nt + row) * (P_ * 2) + kg * 256 + ((r ^ row) << 4), 0);
      }
    }
  }
}

DN void faf_poll(rsrc_t rws, char* sm, int rt, int l, u64 tag) {
  __builtin_amdgcn_s_sleep(8);
  flag_wait(rws, WS_FAF + (rt * 32 + (l & 31)) * 8, l < 18, tag);
  lds_st(sm + SY_P2GO + 8, 1u);
}
DN void p2_helper(const Args& a, char* sm, int ti, int tj, int rt, int cg, int hw, int l, const Early& y) {
  lds_wait(sm + SY_GATH, 1u, lds_ld<u32x2>(sm + SY_GATH));
  const u64 tag = *(const u64*)(sm + SY_TAG);
  const rsrc_t rws = mk_rsrc(a.ws, WS_END);
  const int ht = hw * 64 + l;
  if (tj >= 9) {
    const int jb = tj - 9, wn = hw & 1, rj = 2 * ti + (hw >> 1), ct0 = 6 * jb + 3 * wn, c16 = l & 15, g = l >> 4;
    const int nct = (jb == 6 && wn == 1) ? 1 : 3;
    cpx lg[3], l8[3];
    f32x2 cf[3], wk[3][8];
    const cpx one = {1.f, 0.f};
#pragma unroll
    for (int c = 0; c < 3; ++c) {
      const char* lp = sm + LD_LAM + (16 * (3 * wn + c) + c16) * 48;
      const f32x4 t0 = *(const f32x4*)lp, t1 = *(const f32x4*)(lp + 16);
      const cpx l32 = to_c(*(const f32x2*)(lp + 32));
      cf[c] = hi2(t0);
      l8[c] = to_c(lo2(t1));
      lg[c] = c_mul(c_sel(g & 1, to_c(hi2(t1)), one), c_sel(g & 2, l32, one));
      cpx t = to_c(cf[c]);
#pragma unroll
      for (int k = 0; k < 8; ++k) {
        t = c_mul(to_c(lo2(t0)), t);
        wk[c][k] = (f32x2){t.re, t.im};
        asm volatile("" : "+v"(wk[c][k]));
      }
      asm volatile("" : "+v"(lg[c].re), "+v"(lg[c].im));
    }
    lds_wait(sm + SY_FREE + 4 * hw, 11u, lds_ld<unsigned>(sm + SY_FREE + 4 * hw));
    __builtin_amdgcn_s_setprio(3);
    f32x2 bre[3][8], bim[3][8];
    f32x4 pe[3];
#pragma unroll
    for (int c = 0; c < 3; ++c) {
      const char* jt = job_tile(sm, 3 * hw + c);
      u32x4 h[4];
#pragma unroll
      for (int q = 0; q < 4; ++q) h[q] = *(const u32x4*)(jt + q * 1024 + l * 16);
      pe[c] = *(const f32x4*)(jt + 4096 + l * 16);
#pragma unroll
      for (int k = 0; k < 8; ++k) {
        const unsigned ur = h[k >> 2][k & 3], ui = h[2 + (k >> 2)][k & 3];
        const f32x2 hr = {lo_f(ur), hi_f(ur)}, hi = {lo_f(ui), hi_f(ui)};
        bre[c][k] = pk_fma_b<1, 1>(cf[c], hi, pk_mul_b<0, 0>(cf[c], hr));
        bim[c][k] = pk_fma_b<1, 1>(cf[c], hr, pk_mul_b<0, 1>(cf[c], hi));
        asm volatile("" : "+v"(bre[c][k]), "+v"(bim[c][k]));
      }
    }
    SB();
    {
      if (hw == 2) faf_poll(rws, sm, rt, l, tag);
      lds_wait(sm + SY_CAR + 4 * hw, 1u, lds_ld<unsigned>(sm + SY_CAR + 4 * hw));
#pragma unroll
      for (int c = 0; c < 3; ++c) {
        const cpx H = to_c(*(const f32x2*)(sm + LD_CAR + ((3 * hw + c) * 16 + c16) * 8));
        const cpx ca = c_fma(lg[c], H, to_c(lo2(pe[c]))), cb = c_fma(l8[c], ca, to_c(hi2(pe[c])));
        const f32x2 cre = {ca.re, cb.re}, cim = {ca.im, cb.im};
        f32x2 ore[8], oim[8];
#pragma unroll
        for (int k = 0; k < 8; ++k) {
          ore[k] = pk_fma_b<1, 1>(wk[c][k], cim, pk_fma_b<0, 0>(wk[c][k], cre, bre[c][k]));
          oim[k] = pk_fma_b<1, 1>(wk[c][k], cre, pk_fma_b<0, 1>(wk[c][k], cim, bim[c][k]));
        }
        char* jt = job_tile(sm, 3 * hw + c);
#pragma unroll
        for (int q = 0; q < 2; ++q) {
          u32x4 o0, o1;
#pragma unroll
          for (int i = 0; i < 4; ++i) {
            o0[i] = pk_bf16(ore[2 * i][q], ore[2 * i + 1][q]);
            o1[i] = pk_bf16(oim[2 * i][q], oim[2 * i + 1][q]);
          }
          *(u32x4*)(jt + c16 * 144 + g * 32 + q * 16) = o0;
          *(u32x4*)(jt + (16 + c16) * 144 + g * 32 + q * 16) = o1;
        }
        SB();
      }
#pragma unroll
      for (int c = 0; c < 3; ++c) {
        const unsigned b = lds_addr(job_tile(sm, 3 * hw + c)) + (8 * g + (c16 >> 2)) * 144 + (c16 & 3) * 8;
        u32x4 v[4];
        tr4(b, v);
#pragma unroll
        for (int mt = 0; mt < 4; ++mt)
          __builtin_amdgcn_raw_buffer_store_b128(
              v[mt], rws, (c < nct ? WS_HF + ((rj * KSY + ct0 + c) * 4 + mt) * 1024 : WS_END) + l * 16, 0, SC1);
      }
    }
    __builtin_amdgcn_s_setprio(0);
    WAIT_VM(0);
    if (l == 0) flag_st(rws, WS_FH + (rj * 32 + 2 * jb + wn) * 8, tag);
  } else if (hw >= 2) {
    const int job = (ti * 9 + tj) * 2 + (hw - 2), r = l & 15, g = l >> 4;
    if (job < 200) {
      const int pcg = job & 7, q = job >> 3, nt = q / 5, kg = q - 5 * nt;
      const char* b = sm + LD_AUX + (hw - 2) * 8192 + (g >> 1) * 4096 + r * 256;
      u32x4 vc[8];
      WAIT_VM(0);
#pragma unroll
      for (int e = 0; e < 8; ++e) vc[e] = *(const u32x4*)(b + (((2 * e + (g & 1)) ^ r) << 4));
      lds_wait<u32x4, 8>(sm + SY_FREE, 11u, lds_ld<u32x4>(sm + SY_FREE));
#pragma unroll
      for (int e = 0; e < 8; ++e)
        __builtin_amdgcn_raw_buffer_store_b128(vc[e], rws, WS_CF + ((pcg * KSY + 8 * kg + e) * 5 + nt) * 1024 + l * 16, 0, SC1);
      WAIT_VM(0);
      if (l == 0) flag_st(rws, WS_FPC + (pcg * 32 + q) * 8, tag);
    }
  } else if (hw == 1) {
    lds_wait(sm + SY_FREE + 4 * hw, 11u, lds_ld<unsigned>(sm + SY_FREE + 4 * hw));
    faf_poll(rws, sm, rt, l, tag);
  }
  auto y_poll = [&]() {
    lds_wait<unsigned, 8>(sm + SY_YRDY + 4, 1u, lds_ld<unsigned>(sm + SY_YRDY + 4));
    const unsigned fp = (l < 16 ? WS_FH + (rt * 32 + l) * 8 : WS_FPC + cg * 256 + (l - 16) * 8);
    flag_wait(rws, fp, l < 14 || (l >= 16 && l < 41), tag);
    lds_st(sm + SY_YRDY, 1u);
  };
  if (hw == 0 && tj >= 9) y_poll();
  float xs[5][4], xd[5][4], ri[5];
#pragma unroll
  for (int e = 0; e < 5; ++e) {
    const int row = (ht + 256 * e) / 20;
    const f32x2 st = *(const f32x2*)(sm + LD_ST2 + row * 8);
    xs[e][0] = lo_f(y.xv[e][0]); xs[e][1] = hi_f(y.xv[e][0]); xs[e][2] = lo_f(y.xv[e][1]); xs[e][3] = hi_f(y.xv[e][1]);
    const float ds[4] = {lo_f(y.dv[e][0]), hi_f(y.dv[e][0]), lo_f(y.dv[e][1]), hi_f(y.dv[e][1])};
    const float mb = st[0];
    ri[e] = 1.f / st[1];
#pragma unroll
    for (int i = 0; i < 4; ++i) xd[e][i] = rbf(rbf(rbf(xs[e][i] - mb) * ri[e]) * ds[i]);
    asm volatile("" : "+v"(xd[e][0]), "+v"(xd[e][1]), "+v"(xd[e][2]), "+v"(xd[e][3]), "+v"(ri[e]));
  }
  if (hw == 0 && tj < 9) y_poll();
  if (blockIdx.x == 0 && hw == 1) {
#pragma unroll
    for (int i = 0; i < 4; ++i) {
      const unsigned fp = WS_FST + (i * 64 + l) * 8;
      flag_wait(rws, fp, true, tag);
    }
    if (l == 0) flag_st(rws, WS_EP, tag);
  }
  int po[5], oo[5];
#pragma unroll
  for (int e = 0; e < 5; ++e) {
    const int idx = ht + 256 * e, row = idx / 20, c4 = idx - row * 20;
    po[e] = LD_P2 + row * RED_ROW + c4 * 16;
    oo[e] = ((rt * 64 + row) * D_ + cg * 80 + c4 * 4) * 4;
    asm volatile("" : "+v"(po[e]), "+v"(oo[e]));
  }
  auto sum4 = [&](f32x4 (&t)[5]) {
    f32x4 p[5][4];
#pragma unroll
    for (int e = 0; e < 5; ++e)
#pragma unroll
      for (int q = 0; q < 4; ++q) p[e][q] = *(const f32x4*)(sm + po[e] + q * WREG);
    SB();
#pragma unroll
    for (int e = 0; e < 5; ++e) t[e] = (p[e][0] + p[e][1]) + (p[e][2] + p[e][3]);
  };
  bar();
  f32x4 gt[5], base[5];
  {
    f32x4 f[5];
    sum4(f);
    float fv[5][4];
#pragma unroll
    for (int e = 0; e < 5; ++e) {
      const f32x2 rr = {ri[e], ri[e]};
      const f32x2 a0 = pk_mul(lo2(f[e]), rr), a1 = pk_mul(hi2(f[e]), rr);
      const unsigned u0 = pk_bf16(a0[0], a0[1]), u1 = pk_bf16(a1[0], a1[1]);
      fv[e][0] = lo_f(u0); fv[e][1] = hi_f(u0); fv[e][2] = lo_f(u1); fv[e][3] = hi_f(u1);
    }
    float sg[5][4];
#pragma unroll
    for (int e = 0; e < 5; ++e)
#pragma unroll
      for (int i = 0; i < 4; ++i) sg[e][i] = __builtin_amdgcn_exp2f(mul_(fv[e][i], -1.44269504088896f));
    SB();
#pragma unroll
    for (int e = 0; e < 5; ++e)
#pragma unroll
      for (int i = 0; i < 4; ++i) sg[e][i] = __builtin_amdgcn_rcpf(1.f + sg[e][i]);
    SB();
#pragma unroll
    for (int e = 0; e < 5; ++e) {
      const unsigned u0 = pk_bf16(fv[e][0] * sg[e][0], fv[e][1] * sg[e][1]);
      const unsigned u1 = pk_bf16(fv[e][2] * sg[e][2], fv[e][3] * sg[e][3]);
      gt[e] = (f32x4){lo_f(u0), hi_f(u0), lo_f(u1), hi_f(u1)};
      base[e] = (f32x4){xs[e][0], xs[e][1], xs[e][2], xs[e][3]};
      asm volatile("" : "+v"(gt[e]), "+v"(base[e]));
    }
  }
  bar();
  bar();
  {
    f32x4 y4[5];
    sum4(y4);
    const rsrc_t ro = mk_rsrc(a.out, L_ * D_ * 4);
#pragma unroll
    for (int e = 0; e < 5; ++e) {
      const f32x4 sv = y4[e] + (f32x4){xd[e][0], xd[e][1], xd[e][2], xd[e][3]};
      const f32x2 o0 = pk_fma(lo2(sv), lo2(gt[e]), lo2(base[e])), o1 = pk_fma(hi2(sv), hi2(gt[e]), hi2(base[e]));
      const u32x4 o4 = {as_u(o0[0]), as_u(o0[1]), as_u(o1[0]), as_u(o1[1])};
      __builtin_amdgcn_raw_buffer_store_b128(o4, ro, oo[e], 0, 0);
    }
  }
}

__global__ __launch_bounds__(512, 2) void fused_kernel(Args a) {
  extern __shared__ __attribute__((aligned(16))) char sm[];
  const int tid = threadIdx.x, l = tid & 63;
  const int w = __builtin_amdgcn_readfirstlane(tid >> 6);
  const int b = blockIdx.x, x = b & 7, o = b >> 3;
  const int ti = (x & 1) * 8 + (o & 7), tj = (x >> 1) + 4 * (o >> 3);
  const int rt = x * 4 + (o >> 3), cg = o & 7;
  {
    u64 pc;
    asm volatile("s_getpc_b64 %0" : "=s"(pc));
    if (w == 3)
      __builtin_amdgcn_raw_ptr_buffer_load_lds(mk_rsrc((const void*)pc, CODEPF), (LDS_AS void*)(sm + LD_SCR), 4,
                                               (o * 64 + l) * 64, 0, 0, 0);
  }
  if (__builtin_expect(w < 4, 0)) {
    __builtin_amdgcn_s_setprio(2);
    if (tj < 9) p1_gemm<false>(a, sm, ti, tj, w, l);
    else p1_gemm<true>(a, sm, ti, tj, w, l);
    p2_gemm(a, sm, rt, cg, w, l);
  } else {
    Early y;
    if (__builtin_expect(w < 6, 1)) {
      p1_loader(a, sm, ti, tj, rt, cg, w - 4, l, y);
    } else {
      if (tj < 9) p1_side<false>(a, sm, ti, tj, rt, cg, w - 6, l);
      else p1_side<true>(a, sm, ti, tj, rt, cg, w - 6, l);
      early_loads(a, rt, cg, w - 4, l, y);
    }
    __builtin_amdgcn_s_setprio(0);
    p2_helper(a, sm, ti, tj, rt, cg, w - 4, l, y);
  }
}

extern "C" void kernel_launch(void* const* d_in, const int* in_sizes, int n_in, void* d_out, int out_size,
                              void* d_ws, size_t ws_size, hipStream_t stream) {
  (void)in_sizes; (void)n_in; (void)out_size; (void)ws_size;
  Args a;
  a.x = (const u16*)d_in[0]; a.lre = (const u16*)d_in[1]; a.lim = (const u16*)d_in[2];
  a.bre = (const u16*)d_in[3]; a.bim = (const u16*)d_in[4]; a.cre = (const u16*)d_in[5];
  a.cim = (const u16*)d_in[6]; a.ldt = (const u16*)d_in[7]; a.dsk = (const u16*)d_in[8];
  a.w1 = (const u16*)d_in[10]; a.w2 = (const u16*)d_in[11];
  a.ws = (char*)d_ws; a.out = (float*)d_out;
  (void)hipFuncSetAttribute((const void*)fused_kernel, hipFuncAttributeMaxDynamicSharedMemorySize, LDS_BYTES);
  fused_kernel<<<256, 512, LDS_BYTES, stream>>>(a);
}
```
